```python
import torch

def _softmax(x, axis=-1):
    return torch.softmax(x, dim=axis)
def _gelu(x):
    return torch.nn.functional.gelu(x, approximate="tanh")
def _silu(x):
    return torch.nn.functional.silu(x)
def _sigmoid(x):
    return torch.sigmoid(x)
def _layernorm(x, axis=-1, eps=1e-5):
    m = x.mean(dim=axis, keepdim=True)
    v = x.var(dim=axis, keepdim=True, unbiased=False)
    return (x - m) / torch.sqrt(v + eps)
def _scatter_add(tgt, idx, src, axis=0):
    return tgt.clone().index_add_(axis, idx, src)
def _segment_sum(src, seg, n):
    out = torch.zeros((n,) + src.shape[1:], dtype=src.dtype)
    return out.index_add_(0, seg, src)
def _pad(x, pw):
    flat = tuple(v for pair in reversed(pw) for v in pair)
    return torch.nn.functional.pad(x, flat)

kernel_name = 'gated_mix__sliding_block_cached__glu_gated_swiglu__decode'

def setup_inputs():

    _w = (lambda a: (lambda t: t.to(torch.bfloat16) if t.is_floating_point() else t)(torch.as_tensor(a)))
    def _A_setup():
        B, Sq, D = 4, 1, 512
        Hq, Hkv, Sk, Dh = 32, 8, 8192, 128
        assert Hq % Hkv == 0, (Hq, Hkv)
        return {
            "x": _w((lambda *s: torch.randn(*s, dtype=torch.bfloat16) * 0.05)(B, Sq, D)),
            "k_cache": _w((lambda *s: torch.randn(*s, dtype=torch.bfloat16) * 6.4)(B, Hkv, Sk, Dh)),
            "v_cache": _w((lambda *s: torch.randn(*s, dtype=torch.bfloat16) * 0.05)(B, Hkv, Sk, Dh)),
            "wq": _w((lambda *s: torch.randn(*s, dtype=torch.bfloat16) * 6.4)(D, Hq * Dh) * (D ** -0.5)),
            "wo": _w((lambda *s: torch.randn(*s, dtype=torch.bfloat16) * 0.05)(Hq * Dh, D) * ((Hq * Dh) ** -0.5)),
        }
    _A_in = _A_setup()

    def _B_setup():
        B, S, D, F = 4, 1, 512, 1792
        return {
            "x": _w((lambda *s: torch.randn(*s, dtype=torch.bfloat16) * 0.05)(B, S, D)),
            "w1": _w((lambda *s: torch.randn(*s, dtype=torch.bfloat16) * 0.4)(D, F) * (2.0 / D) ** 0.5),
            "w3": _w((lambda *s: torch.randn(*s, dtype=torch.bfloat16) * 0.4)(D, F) * (2.0 / D) ** 0.5),
            "w2": _w((lambda *s: torch.randn(*s, dtype=torch.bfloat16) * 0.4)(F, D) * (2.0 / F) ** 0.5),
        }
    _B_in = _B_setup()

    return {
        "A_x": _A_in["x"],
        "A_k_cache": _A_in["k_cache"],
        "A_v_cache": _A_in["v_cache"],
        "A_wq": _A_in["wq"],
        "A_wo": _A_in["wo"],
        "B_x": _B_in["x"],
        "B_w1": _B_in["w1"],
        "B_w3": _B_in["w3"],
        "B_w2": _B_in["w2"],
        "gate_w": _w((lambda *s: torch.randn(*s, dtype=torch.bfloat16) * 0.05)(512) * 16.0),
    }

def reference(A_x, A_k_cache, A_v_cache, A_wq, A_wo, B_x, B_w1, B_w3, B_w2, gate_w):
    def _A_ref(x, k_cache, v_cache, wq, wo):
        B, Sq, D = x.shape
        Hq, Dh = 32, 128
        Hkv, Sk = k_cache.shape[1], k_cache.shape[2]
        W = min(4096, max(256, Sk // 4))
        rep = Hq // Hkv
        q = (lambda x, *d: x.permute(*d))((x @ wq).reshape(B, Sq, Hq, Dh), 0, 2, 1, 3)
        k = (lambda x, n, axis: torch.repeat_interleave(x, n, dim=axis))(k_cache[:, :, Sk - W:, :], rep, axis=1)
        v = (lambda x, n, axis: torch.repeat_interleave(x, n, dim=axis))(v_cache[:, :, Sk - W:, :], rep, axis=1)
        scores = torch.einsum("bhqd,bhkd->bhqk", q, k) * (Dh ** -0.5)
        p = _softmax(scores, axis=-1)
        o = torch.einsum("bhqk,bhkd->bhqd", p, v)
        return (lambda x, *d: x.permute(*d))(o, 0, 2, 1, 3).reshape(B, Sq, Hq * Dh) @ wo

    def _B_ref(x, w1, w3, w2):
        gate = torch.einsum("bsd,df->bsf", x, w1)
        up = torch.einsum("bsd,df->bsf", x, w3)
        h = _silu(gate) * up
        out = torch.einsum("bsf,fd->bsd", h, w2)
        return out

    x = A_x
    g = _sigmoid((x * gate_w).sum(-1, keepdims=True))
    a = _A_ref(x, A_k_cache, A_v_cache, A_wq, A_wo)
    b = _B_ref(x, B_w1, B_w3, B_w2)
    return g * a + (1 - g) * b
```

```cpp
#include <hip/hip_runtime.h>
#include <hip/hip_bf16.h>
#include <stdint.h>

typedef unsigned short u16;
__device__ __forceinline__ float bf2f(u16 v) { return __uint_as_float(((unsigned)v) << 16); }
__device__ __forceinline__ u16 f2bf(float f) { __hip_bfloat16 h = __float2bfloat16(f); return *(u16*)&h; }
__device__ __forceinline__ float rbf(float f) { return bf2f(f2bf(f)); }

constexpr int B = 4, D = 512, HQ = 32, HKV = 8, SK = 8192, DH = 128, W = 2048, F = 1792;
constexpr int NS = 8;

__global__ void k1(const u16* x, const u16* wq, const u16* w1, const u16* w3, float* q, float* h) {
  __shared__ float xs[B * D];
  for (int i = threadIdx.x; i < B * D; i += blockDim.x) xs[i] = bf2f(x[i]);
  __syncthreads();
  int c = blockIdx.x * blockDim.x + threadIdx.x;
  if (c < 4096) {
    float a[4] = {0, 0, 0, 0};
    for (int k = 0; k < D; ++k) { float w = bf2f(wq[(size_t)k * 4096 + c]);
      for (int b = 0; b < 4; ++b) a[b] += xs[b * D + k] * w; }
    for (int b = 0; b < 4; ++b) q[b * 4096 + c] = rbf(a[b]);
  } else if (c < 4096 + F) {
    int f = c - 4096;
    float g[4] = {0, 0, 0, 0}, u[4] = {0, 0, 0, 0};
    for (int k = 0; k < D; ++k) { float a = bf2f(w1[(size_t)k * F + f]), bb = bf2f(w3[(size_t)k * F + f]);
      for (int b = 0; b < 4; ++b) { g[b] += xs[b * D + k] * a; u[b] += xs[b * D + k] * bb; } }
    for (int b = 0; b < 4; ++b) { float gg = g[b]; float s = gg / (1.f + __expf(-gg)); h[b * F + f] = s * u[b]; }
  }
}

__global__ void k2(const u16* kc, const u16* vc, const float* q, float* po, float* pml) {
  __shared__ float qs[4 * DH];
  __shared__ float p[4][256];
  __shared__ float red[4][4];
  int s = blockIdx.x, kvh = blockIdx.y, b = blockIdx.z, t = threadIdx.x;
  for (int i = t; i < 512; i += 256) qs[i] = q[b * 4096 + kvh * 512 + i];
  __syncthreads();
  size_t base = ((size_t)(b * HKV + kvh) * SK + (SK - W) + s * 256) * DH;
  const u16* kr = kc + base + (size_t)t * DH;
  float sc[4] = {0, 0, 0, 0};
  for (int d = 0; d < DH; ++d) { float kv = bf2f(kr[d]); for (int g = 0; g < 4; ++g) sc[g] += kv * qs[g * DH + d]; }
  float m[4];
  for (int g = 0; g < 4; ++g) { sc[g] *= 0.08838834764831845f; float v = sc[g];
    for (int o = 32; o; o >>= 1) v = fmaxf(v, __shfl_xor(v, o));
    if ((t & 63) == 0) red[g][t >> 6] = v; }
  __syncthreads();
  for (int g = 0; g < 4; ++g) m[g] = fmaxf(fmaxf(red[g][0], red[g][1]), fmaxf(red[g][2], red[g][3]));
  __syncthreads();
  for (int g = 0; g < 4; ++g) { float e = __expf(sc[g] - m[g]); p[g][t] = e; float v = e;
    for (int o = 32; o; o >>= 1) v += __shfl_xor(v, o);
    if ((t & 63) == 0) red[g][t >> 6] = v; }
  __syncthreads();
  int d = t & 127, gh = t >> 7;
  float a0 = 0, a1 = 0;
  for (int k = 0; k < 256; ++k) { float v = bf2f(vc[base + (size_t)k * DH + d]); a0 += p[gh * 2][k] * v; a1 += p[gh * 2 + 1][k] * v; }
  size_t r0 = ((size_t)(b * HQ + kvh * 4 + gh * 2) * NS + s), r1 = r0 + NS;
  po[r0 * DH + d] = a0; po[r1 * DH + d] = a1;
  if (t < 4) { size_t r = ((size_t)(b * HQ + kvh * 4 + t) * NS + s); pml[r * 2] = m[t]; pml[r * 2 + 1] = red[t][0] + red[t][1] + red[t][2] + red[t][3]; }
}

__global__ void k3(const float* po, const float* pml, float* o) {
  int row = blockIdx.x; int d = threadIdx.x;
  float M = -INFINITY; for (int s = 0; s < NS; ++s) M = fmaxf(M, pml[(row * NS + s) * 2]);
  float L = 0, a = 0;
  for (int s = 0; s < NS; ++s) { float w = __expf(pml[(row * NS + s) * 2] - M); L += w * pml[(row * NS + s) * 2 + 1]; a += w * po[(size_t)(row * NS + s) * DH + d]; }
  o[row * DH + d] = rbf(a / L);
}

__global__ void k4(const u16* x, const u16* gw, const float* o, const float* h, const u16* wo, const u16* w2, u16* out) {
  __shared__ float ra[4][64], rb[4][64];
  __shared__ float gs;
  int b = blockIdx.y, c = blockIdx.x * 64 + (threadIdx.x & 63), kg = threadIdx.x >> 6;
  float a = 0, f = 0;
  for (int k = kg; k < 4096; k += 4) a += o[b * 4096 + k] * bf2f(wo[(size_t)k * D + c]);
  for (int k = kg; k < F; k += 4) f += rbf(h[b * F + k]) * bf2f(w2[(size_t)k * D + c]);
  ra[kg][threadIdx.x & 63] = a; rb[kg][threadIdx.x & 63] = f;
  if (threadIdx.x < 64) { float s = 0; for (int k = threadIdx.x; k < D; k += 64) s += bf2f(x[b * D + k]) * bf2f(gw[k]);
    for (int o2 = 32; o2; o2 >>= 1) s += __shfl_xor(s, o2);
    if (threadIdx.x == 0) gs = 1.f / (1.f + __expf(-s)); }
  __syncthreads();
  if (threadIdx.x < 64) { int t = threadIdx.x; float A = ra[0][t] + ra[1][t] + ra[2][t] + ra[3][t], Bv = rb[0][t] + rb[1][t] + rb[2][t] + rb[3][t];
    out[b * D + c] = f2bf(gs * A + (1.f - gs) * Bv); }
}

extern "C" void kernel_launch(void* const* d_in, const int* in_sizes, int n_in, void* d_out, int out_size, void* d_ws, size_t ws_size, hipStream_t stream) {
  const u16* x = (const u16*)d_in[0]; const u16* kc = (const u16*)d_in[1]; const u16* vc = (const u16*)d_in[2];
  const u16* wq = (const u16*)d_in[3]; const u16* wo = (const u16*)d_in[4];
  const u16* w1 = (const u16*)d_in[6]; const u16* w3 = (const u16*)d_in[7]; const u16* w2 = (const u16*)d_in[8]; const u16* gw = (const u16*)d_in[9];
  float* ws = (float*)d_ws;
  float* q = ws; float* h = q + 4 * 4096; float* po = h + 4 * 2048; float* pml = po + 128 * NS * DH; float* o = pml + 128 * NS * 2;
  k1<<<(4096 + F + 63) / 64, 64, 0, stream>>>(x, wq, w1, w3, q, h);
  k2<<<dim3(NS, HKV, B), 256, 0, stream>>>(kc, vc, q, po, pml);
  k3<<<128, 128, 0, stream>>>(po, pml, o);
  k4<<<dim3(8, 4), 256, 0, stream>>>(x, gw, o, h, wo, w2, (u16*)d_out);
}
```

```cpp
#include <hip/hip_runtime.h>
#include <stdint.h>

typedef unsigned u32;
typedef unsigned long long u64;
typedef unsigned short u16;
typedef u32 v4u __attribute__((ext_vector_type(4)));
typedef u32 v2u __attribute__((ext_vector_type(2)));
typedef float f32x4 __attribute__((ext_vector_type(4)));
typedef float f32x2 __attribute__((ext_vector_type(2)));
typedef __bf16 bf16x8 __attribute__((ext_vector_type(8)));
typedef __bf16 bf16x2 __attribute__((ext_vector_type(2)));
typedef short s16x4 __attribute__((ext_vector_type(4)));
#define GAS __attribute__((address_space(1)))
#define LAS __attribute__((address_space(3)))
#define RLX_AGENT __ATOMIC_RELAXED, __HIP_MEMORY_SCOPE_AGENT

#define KV_AUX 2
#define W_NT 0
#define CODE_LINES 58

constexpr u32 WS_Q = 64, WS_H = WS_Q + 32768, WS_O = WS_H + 32768, WS_ML = WS_O + 65536, WS_R = WS_ML + 2048, WS_END = WS_R + 65536;
constexpr u32 SPIN_CAP = 1u << 16;
constexpr int L_RED = 0, L_QS = 8192, L_MX = L_QS + 1024, L_LS = L_MX + 64, L_OA = L_LS + 64, L_HA = L_OA + 4 * 272, L_W = L_HA + 4 * 144, WSTR = 144,
              L_END = L_W + 192 * WSTR;

#define SB() __builtin_amdgcn_sched_barrier(0)
#define BAR() asm volatile("s_waitcnt lgkmcnt(0)\n\ts_barrier" ::: "memory")

__device__ __forceinline__ float asf(u32 x) { return __builtin_bit_cast(float, x); }
__device__ __forceinline__ u32 asu(float x) { return __builtin_bit_cast(u32, x); }
__device__ __forceinline__ u32 pk_bf16(float lo, float hi) {
  bf16x2 r = {(__bf16)lo, (__bf16)hi};
  return __builtin_bit_cast(u32, r);
}
__device__ __forceinline__ float rbf(float v) { return (float)(__bf16)v; }
template <int CTRL> __device__ __forceinline__ float dpp(float x) {
  return __builtin_bit_cast(float, __builtin_amdgcn_mov_dpp(__builtin_bit_cast(int, x), CTRL, 0xf, 0xf, true));
}
__device__ __forceinline__ float x16_32_max(float x) {
  auto s = __builtin_amdgcn_permlane16_swap(asu(x), asu(x), false, false);
  x = fmaxf(asf(s[0]), asf(s[1]));
  auto t = __builtin_amdgcn_permlane32_swap(asu(x), asu(x), false, false);
  return fmaxf(asf(t[0]), asf(t[1]));
}
__device__ __forceinline__ float x16_32_sum(float x) {
  auto s = __builtin_amdgcn_permlane16_swap(asu(x), asu(x), false, false);
  x = asf(s[0]) + asf(s[1]);
  auto t = __builtin_amdgcn_permlane32_swap(asu(x), asu(x), false, false);
  return asf(t[0]) + asf(t[1]);
}
__device__ __forceinline__ f32x4 mfma(v4u a, v4u b, f32x4 c) {
  return __builtin_amdgcn_mfma_f32_16x16x32_bf16(__builtin_bit_cast(bf16x8, a), __builtin_bit_cast(bf16x8, b), c, 0, 0, 0);
}
template <int I> __device__ __forceinline__ v4u colfrag(const v4u* r) {
  constexpr u32 sel = (I & 1) ? 0x07060302u : 0x05040100u;
  v4u f;
#pragma unroll
  for (int d = 0; d < 4; ++d) f[d] = __builtin_amdgcn_perm(r[2 * d + 1][I >> 1], r[2 * d][I >> 1], sel);
  return f;
}
__device__ __forceinline__ u64 pack2(float a, float b, u32 tag) {
  u32 lo = __builtin_amdgcn_perm(asu(b), asu(a), 0x05030201u), hi = __builtin_amdgcn_perm(tag, asu(b), 0x05040302u);
  return ((u64)hi << 32) | lo;
}
__device__ __forceinline__ float unp_a(u64 g) { return asf((u32)g << 8); }
__device__ __forceinline__ float unp_b(u64 g) { return asf(__builtin_amdgcn_perm((u32)(g >> 32), (u32)g, 0x0504030cu)); }
__device__ __forceinline__ u64 gld(const u64* p) { return __hip_atomic_load((GAS u64*)p, RLX_AGENT); }
__device__ __forceinline__ void gst(u64* p, u64 v) { __hip_atomic_store((GAS u64*)p, v, RLX_AGENT); }
template <int N> __device__ __forceinline__ bool tags_ok(const u64* gr, u32 tagh) {
  u32 bad = 0;
#pragma unroll
  for (int k = 0; k < N; ++k) bad |= (u32)(gr[k] >> 32) ^ tagh;
  return __all(bad < 65536u);
}
__device__ __forceinline__ v4u ldw(const char* p) {
  return *(const GAS v4u*)p;
}
#define ST(k)

__global__ void __launch_bounds__(256, 2)
fused_decode(const char* __restrict__ x, const char* __restrict__ kc, const char* __restrict__ vc, const char* __restrict__ wq,
             const char* __restrict__ wo, const char* __restrict__ w1, const char* __restrict__ w3, const char* __restrict__ w2,
             const char* __restrict__ gw, u16* __restrict__ out, u64* __restrict__ ws) {
  __shared__ __attribute__((aligned(16))) char lds[L_END];
  ST(0);
  const int tid = threadIdx.x, l = tid & 63, w = __builtin_amdgcn_readfirstlane(tid >> 6);
  const int G = blockIdx.x & 7, r = blockIdx.x >> 3, rh = r >> 3, rl = r & 7;
  const int g = l >> 4;
  const bool ffn = G < 7;

  const size_t kvoff = (size_t)((rh * 8 + G) * 8192 + 6144) * 256;
  const __amdgpu_buffer_rsrc_t rk = __builtin_amdgcn_make_buffer_rsrc((void*)(kc + kvoff), 0, 2048 * 256, 0x00020000);
  const __amdgpu_buffer_rsrc_t rv = __builtin_amdgcn_make_buffer_rsrc((void*)(vc + kvoff), 0, 2048 * 256, 0x00020000);
  const u32 koff = ((l >> 2) & 3) * 256 + g * 64 + (l & 3) * 16, voff = l * 16;
  const u32 sbase = (rl * 4 + w) * 1024;
  v4u K[16], V[16];
#define LDK(j) K[j] = __builtin_amdgcn_raw_buffer_load_b128(rk, koff, sbase + (j) * 32768, KV_AUX)
#define LDV(j) V[j] = __builtin_amdgcn_raw_buffer_load_b128(rv, voff, sbase + (j) * 32768, KV_AUX)
  asm volatile("" ::"s"(x), "s"(wq), "s"(w1), "s"(w3), "s"(gw), "s"(ws), "s"(kc));
  LDK(0); LDK(1);
  SB();
  const u32 epoch_v = __hip_atomic_load((GAS u32*)ws, RLX_AGENT);
  const u64 pc = __builtin_amdgcn_s_getpc() & ~127ull;
  const u32 codew = *(const GAS u32*)(pc + (u64)(min(r + 32 * (tid & 1), CODE_LINES - 1) * 128));
  const int krow = 128 * rh + 32 * w + 8 * g;
  const v4u xa = *(const GAS v4u*)(x + (((l & 15) >> 2) * 512 + krow) * 2);
  v4u S1[8];
  {
    const bool m1 = ((l >> 3) & 1) && ffn;
    const char* p0 = wq + (size_t)(krow * 8192 + (8 * G + rl) * 128 + (l & 7) * 16);
    const char* p1 = ((rl >> 2) ? w3 : w1) + (size_t)(krow * 3584 + (4 * G + (rl & 3)) * 128 + (l & 7) * 16);
    const char* p = m1 ? p1 : p0;
    const u32 str = m1 ? 3584u : 8192u;
#pragma unroll
    for (int j = 0; j < 8; ++j) S1[j] = ldw(p + (size_t)(j * str));
  }
  const v4u xg = *(const GAS v4u*)(x + (w * 512 + l * 8) * 2);
  const v4u gv = *(const GAS v4u*)(gw + l * 16);
  SB();
  LDK(2); LDK(3); LDK(4); LDK(5); LDK(6); LDK(7); LDK(8); LDK(9);
  SB();
  ST(1);
  const u32 tag = __builtin_amdgcn_readfirstlane(epoch_v) + 1u;
  const u32 tagh = tag << 16;
  asm volatile("" ::"v"(codew));

  {
    const f32x4 z = {0.f, 0.f, 0.f, 0.f};
    f32x4 d0 = mfma(xa, colfrag<0>(S1), z), d1 = mfma(xa, colfrag<1>(S1), z), d2 = mfma(xa, colfrag<2>(S1), z), d3 = mfma(xa, colfrag<3>(S1), z);
    f32x4 d4 = mfma(xa, colfrag<4>(S1), z), d5 = mfma(xa, colfrag<5>(S1), z), d6 = mfma(xa, colfrag<6>(S1), z), d7 = mfma(xa, colfrag<7>(S1), z);
    float* dst = (float*)(lds + L_RED) + ((w * 2 + ((l >> 3) & 1)) * 4 + g) * 64 + (l & 7) * 8;
    *(f32x4*)dst = f32x4{d0[0], d1[0], d2[0], d3[0]};
    *(f32x4*)(dst + 4) = f32x4{d4[0], d5[0], d6[0], d7[0]};
  }
  BAR();
  {
    const float* src = (const float*)(lds + L_RED) + tid * 2;
    f32x2 a = *(const f32x2*)src, b2 = *(const f32x2*)(src + 512), c = *(const f32x2*)(src + 1024), d = *(const f32x2*)(src + 1536);
    const float s0 = (a[0] + b2[0]) + (c[0] + d[0]), s1 = (a[1] + b2[1]) + (c[1] + d[1]);
    const int bb = (tid >> 5) & 3, pr = tid & 31;
    const u32 iq = WS_Q + (((G * 4 + bb) * 4 + rh) * 8 + rl) * 32 + pr;
    const u32 ih = WS_H + ((((G * 4 + (rl & 3)) * 4 + bb) * 4 + rh) * 2 + (rl >> 2)) * 32 + pr;
    gst(ws + (w < 2 ? iq : ih), pack2(s0, s1, tag));
  }
  SB();
  ST(2);
  LDK(10); LDK(11); LDK(12); LDK(13); LDK(14); LDK(15);
  LDV(0); LDV(1); LDV(2); LDV(3);
  SB();
  float gate;
  {
    float s = 0.f;
#pragma unroll
    for (int d = 0; d < 4; ++d) {
      s += rbf(asf(xg[d] << 16) * asf(gv[d] << 16));
      s += rbf(asf(xg[d] & 0xffff0000u) * asf(gv[d] & 0xffff0000u));
    }
    s += dpp<0xB1>(s); s += dpp<0x4E>(s); s += dpp<0x141>(s); s += dpp<0x128>(s);
    s = rbf(x16_32_sum(s));
    gate = rbf(1.f / (1.f + __expf(-s)));
  }
  SB();
  const u64* qp = ws + WS_Q + (G * 4 + rh) * 1024 + tid;
  u64 gq[4];
#pragma unroll
  for (int k = 0; k < 4; ++k) gq[k] = gld(qp + k * 256);
  SB();
  LDV(4); LDV(5); LDV(6); LDV(7); LDV(8); LDV(9); LDV(10); LDV(11); LDV(12); LDV(13); LDV(14); LDV(15);
  SB();
  ST(3);
  if (!tags_ok<4>(gq, tagh)) {
    for (u32 spins = 0; spins < SPIN_CAP; ++spins) {
#pragma unroll
      for (int k = 0; k < 4; ++k) gq[k] = gld(qp + k * 256);
      if (tags_ok<4>(gq, tagh)) break;
    }
  }
  ST(4);
  *(u32*)(lds + L_QS + tid * 4) = pk_bf16((unp_a(gq[0]) + unp_a(gq[1])) + (unp_a(gq[2]) + unp_a(gq[3])), (unp_b(gq[0]) + unp_b(gq[1])) + (unp_b(gq[2]) + unp_b(gq[3])));
  BAR();
  v4u P0, P1;
  float mrow;
  {
    const v4u qb = *(const v4u*)(lds + L_QS + ((l >> 2) & 3) * 256 + (4 * g + (l & 3)) * 16);
    const f32x4 z = {0.f, 0.f, 0.f, 0.f};
    float s[16];
#pragma unroll
    for (int j = 0; j < 16; ++j) {
      const f32x4 d = mfma(K[j], qb, z);
      s[j] = (dpp<0x00>(d[0]) + dpp<0x55>(d[1])) + (dpp<0xAA>(d[2]) + dpp<0xFF>(d[3]));
    }
    float m = s[0];
#pragma unroll
    for (int j = 1; j < 16; ++j) m = fmaxf(m, s[j]);
    m = x16_32_max(m);
    if (l < 16) *(float*)(lds + L_MX + (l >> 2) * 16 + w * 4) = m;
    BAR();
    const f32x4 mm = *(const f32x4*)(lds + L_MX + ((l >> 2) & 3) * 16);
    constexpr float C2 = 0.08838834764831845f * 1.4426950408889634f;
    mrow = fmaxf(fmaxf(mm[0], mm[1]), fmaxf(mm[2], mm[3])) * C2;
    float ls = 0.f;
#pragma unroll
    for (int j = 0; j < 16; ++j) { s[j] = __builtin_amdgcn_exp2f(fmaf(s[j], C2, -mrow)); ls += s[j]; }
    ls = x16_32_sum(ls);
    if (l < 16) *(float*)(lds + L_LS + (l >> 2) * 16 + w * 4) = ls;
    P0 = v4u{pk_bf16(s[0], s[1]), pk_bf16(s[2], s[3]), pk_bf16(s[4], s[5]), pk_bf16(s[6], s[7])};
    P1 = v4u{pk_bf16(s[8], s[9]), pk_bf16(s[10], s[11]), pk_bf16(s[12], s[13]), pk_bf16(s[14], s[15])};
  }
  SB();
  ST(5);
  const int h = 4 * G + rh;
  v4u WO[4], W2[2];
  u64 gh[4];
  const u64* hp = ws + WS_H + ((G * 4 + rh) * 4 + w) * 256 + l;
  {
    const char* p = wo + (size_t)((128 * h + 8 * w + (l >> 3)) * 1024 + rl * 128 + (l & 7) * 16);
#pragma unroll
    for (int j = 0; j < 4; ++j) WO[j] = ldw(p + j * 32768);
    const char* p2 = w2 + (size_t)(ffn ? (64 * h + 8 * w + (l >> 3)) * 1024 + rl * 128 + (l & 7) * 16 : 0);
#pragma unroll
    for (int j = 0; j < 2; ++j) W2[j] = ldw(p2 + j * 32768);
#pragma unroll
    for (int k = 0; k < 4; ++k) gh[k] = gld(hp + k * 64);
  }
  SB();
  BAR();
  {
    const f32x4 lv = *(const f32x4*)(lds + L_LS + w * 16);
    const float v = (l & 1) ? (lv[0] + lv[1]) + (lv[2] + lv[3]) : asf(__builtin_amdgcn_readlane(asu(mrow), 4 * w));
    gst(ws + WS_ML + (((G * 4 + w) * 4 + rh) * 8 + rl) * 2 + (l & 1), ((u64)tag << 32) | asu(v));
  }
  SB();
  ST(6);
  {
    const f32x4 z = {0.f, 0.f, 0.f, 0.f};
    f32x4 d0 = mfma(P0, colfrag<0>(V), z), d1 = mfma(P0, colfrag<1>(V), z), d2 = mfma(P0, colfrag<2>(V), z), d3 = mfma(P0, colfrag<3>(V), z);
    f32x4 d4 = mfma(P0, colfrag<4>(V), z), d5 = mfma(P0, colfrag<5>(V), z), d6 = mfma(P0, colfrag<6>(V), z), d7 = mfma(P0, colfrag<7>(V), z);
    SB();
    d0 = mfma(P1, colfrag<0>(V + 8), d0); d1 = mfma(P1, colfrag<1>(V + 8), d1); d2 = mfma(P1, colfrag<2>(V + 8), d2); d3 = mfma(P1, colfrag<3>(V + 8), d3);
    d4 = mfma(P1, colfrag<4>(V + 8), d4); d5 = mfma(P1, colfrag<5>(V + 8), d5); d6 = mfma(P1, colfrag<6>(V + 8), d6); d7 = mfma(P1, colfrag<7>(V + 8), d7);
    float* dst = (float*)(lds + L_RED) + (w * 4 + g) * 128 + (l & 15) * 8;
    *(f32x4*)dst = f32x4{d0[0], d1[0], d2[0], d3[0]};
    *(f32x4*)(dst + 4) = f32x4{d4[0], d5[0], d6[0], d7[0]};
  }
  ST(7);
  BAR();
  {
    const float* src = (const float*)(lds + L_RED) + tid * 2;
    f32x2 a = *(const f32x2*)src, b2 = *(const f32x2*)(src + 512), c = *(const f32x2*)(src + 1024), d = *(const f32x2*)(src + 1536);
    const float s0 = (a[0] + b2[0]) + (c[0] + d[0]), s1 = (a[1] + b2[1]) + (c[1] + d[1]);
    gst(ws + WS_O + (((G * 4 + w) * 4 + rh) * 8 + rl) * 64 + l, pack2(s0, s1, tag));
  }
  SB();
  ST(8);
  {
    char* dst = lds + L_W + (8 * w + (l >> 3)) * WSTR + (l & 7) * 16;
#pragma unroll
    for (int j = 0; j < 4; ++j) *(v4u*)(dst + j * 32 * WSTR) = WO[j];
    if (ffn) {
#pragma unroll
      for (int j = 0; j < 2; ++j) *(v4u*)(dst + (4 + j) * 32 * WSTR) = W2[j];
      if (!tags_ok<4>(gh, tagh)) {
        for (u32 spins = 0; spins < SPIN_CAP; ++spins) {
#pragma unroll
          for (int k = 0; k < 4; ++k) gh[k] = gld(hp + k * 64);
          if (tags_ok<4>(gh, tagh)) break;
        }
      }
      const float a = rbf((unp_a(gh[0]) + unp_a(gh[1])) + (unp_a(gh[2]) + unp_a(gh[3])));
      const float b2 = rbf((unp_b(gh[0]) + unp_b(gh[1])) + (unp_b(gh[2]) + unp_b(gh[3])));
      auto sa = __builtin_amdgcn_permlane32_swap(asu(a), asu(a), false, false);
      auto sb = __builtin_amdgcn_permlane32_swap(asu(b2), asu(b2), false, false);
      const float ga = asf(sa[0]), ua = asf(sa[1]), gb = asf(sb[0]), ub = asf(sb[1]);
      const float og = 1.f - gate;
      const float ha = rbf(rbf(ga / (1.f + __expf(-ga))) * ua) * og, hb = rbf(rbf(gb / (1.f + __expf(-gb))) * ub) * og;
      if (l < 32) *(u32*)(lds + L_HA + w * 144 + l * 4) = pk_bf16(ha, hb);
    }
  }
  const u64* op = ws + WS_O + ((G * 4 + rh) * 4 + w) * 512 + l;
  const u64* mp = ws + WS_ML + ((G * 4 + rh) * 4 + w) * 16 + (l & 15);
  u64 go[8], gm;
  gm = gld(mp);
#pragma unroll
  for (int k = 0; k < 8; ++k) go[k] = gld(op + k * 64);
  SB();
  BAR();
  v4u BW[4];
  f32x4 acc = {0.f, 0.f, 0.f, 0.f};
  {
    const char* src = lds + L_W + (8 * g + ((l & 15) >> 2)) * WSTR + w * 32 + (l & 3) * 8;
#define TRF(ks) ({ v2u a_ = __builtin_bit_cast(v2u, __builtin_amdgcn_ds_read_tr16_b64_v4i16((LAS s16x4*)(src + (ks) * 32 * WSTR))); \
                   v2u b_ = __builtin_bit_cast(v2u, __builtin_amdgcn_ds_read_tr16_b64_v4i16((LAS s16x4*)(src + ((ks) * 32 + 4) * WSTR))); \
                   v4u{a_[0], a_[1], b_[0], b_[1]}; })
#pragma unroll
    for (int ks = 0; ks < 4; ++ks) BW[ks] = TRF(ks);
    if (ffn) {
      const char* ap = lds + L_HA + ((l & 15) >> 2) * 144 + g * 16;
      acc = mfma(*(const v4u*)ap, TRF(4), acc);
      acc = mfma(*(const v4u*)(ap + 64), TRF(5), acc);
    }
  }
  SB();
  ST(9);
  for (u32 spins = 0;; ++spins) {
    u32 bad = ((u32)(gm >> 32) == tag) ? 0u : ~0u;
#pragma unroll
    for (int k = 0; k < 8; ++k) bad |= (u32)(go[k] >> 32) ^ tagh;
    if (__all(bad < 65536u) || spins >= SPIN_CAP) break;
    gm = gld(mp);
#pragma unroll
    for (int k = 0; k < 8; ++k) go[k] = gld(op + k * 64);
  }
  ST(10);
  {
    const float v = asf((u32)gm);
    const float ms = dpp<0xA0>(v), lsv = dpp<0xF5>(v);
    float M = fmaxf(ms, dpp<0x4E>(ms)); M = fmaxf(M, dpp<0x124>(M)); M = fmaxf(M, dpp<0x128>(M));
    const float e = __builtin_amdgcn_exp2f(ms - M);
    float L = e * lsv; L += dpp<0x4E>(L); L += dpp<0x124>(L); L += dpp<0x128>(L);
    const float wt = e * gate / L;
    float o0 = 0.f, o1 = 0.f;
#pragma unroll
    for (int k = 0; k < 8; ++k) {
      const float wk = asf(__builtin_amdgcn_readlane(asu(wt), 2 * k));
      o0 = fmaf(wk, unp_a(go[k]), o0); o1 = fmaf(wk, unp_b(go[k]), o1);
    }
    *(u32*)(lds + L_OA + w * 272 + l * 4) = pk_bf16(o0, o1);
  }
  BAR();
  {
    const char* ap = lds + L_OA + ((l & 15) >> 2) * 272 + g * 16;
#pragma unroll
    for (int ks = 0; ks < 4; ++ks) acc = mfma(*(const v4u*)(ap + ks * 64), BW[ks], acc);
    gst(ws + WS_R + ((rl * 32 + 8 * g + 2 * w + ((l >> 3) & 1)) * 32 + h) * 8 + (l & 7), ((u64)tag << 32) | asu(acc[0]));
  }
  SB();
  ST(11);
  if (w == 0) {
    const u64* rp = ws + WS_R + (rl * 32 + h) * 256 + l;
    u64 gr[4];
    for (u32 spins = 0;; ++spins) {
      bool ok = true;
#pragma unroll
      for (int k = 0; k < 4; ++k) { gr[k] = gld(rp + k * 64); ok &= (u32)(gr[k] >> 32) == tag; }
      if (__all(ok) || spins >= SPIN_CAP) break;
    }
    float s = (asf((u32)gr[0]) + asf((u32)gr[1])) + (asf((u32)gr[2]) + asf((u32)gr[3]));
    s += dpp<0x128>(s);
    s = x16_32_sum(s);
    if (l < 8) out[(h >> 3) * 512 + rl * 64 + (h & 7) * 8 + l] = (u16)pk_bf16(s, s);
    if (blockIdx.x == 0 && l == 0) __hip_atomic_store((GAS u32*)ws, tag, RLX_AGENT);
  }
}

extern "C" void kernel_launch(void* const* d_in, const int* in_sizes, int n_in, void* d_out, int out_size, void* d_ws, size_t ws_size,
                              hipStream_t stream) {
  fused_decode<<<256, 256, 0, stream>>>((const char*)d_in[0], (const char*)d_in[1], (const char*)d_in[2], (const char*)d_in[3], (const char*)d_in[4],
                                        (const char*)d_in[6], (const char*)d_in[7], (const char*)d_in[8], (const char*)d_in[9], (u16*)d_out, (u64*)d_ws);
}
```

```cpp
#define STAMPS 4
#define ENC_K 0
#define ENC_WG 0
#include <hip/hip_runtime.h>
#include <stdint.h>

typedef unsigned u32;
typedef unsigned long long u64;
typedef unsigned short u16;
typedef u32 v4u __attribute__((ext_vector_type(4)));
typedef u32 v2u __attribute__((ext_vector_type(2)));
typedef float f32x4 __attribute__((ext_vector_type(4)));
typedef float f32x2 __attribute__((ext_vector_type(2)));
typedef __bf16 bf16x8 __attribute__((ext_vector_type(8)));
typedef __bf16 bf16x2 __attribute__((ext_vector_type(2)));
typedef short s16x4 __attribute__((ext_vector_type(4)));
#define GAS __attribute__((address_space(1)))
#define LAS __attribute__((address_space(3)))
#define RLX_AGENT __ATOMIC_RELAXED, __HIP_MEMORY_SCOPE_AGENT

#define KV_AUX 2
#define W_NT 1
#define CODE_LINES 58

constexpr u32 WS_Q = 64, WS_H = WS_Q + 32768, WS_O = WS_H + 32768, WS_ML = WS_O + 65536, WS_R = WS_ML + 2048, WS_END = WS_R + 65536;
constexpr u32 SPIN_CAP = 1u << 16;
constexpr int L_RED = 0, L_QS = 8192, L_MX = L_QS + 1024, L_LS = L_MX + 64, L_OA = L_LS + 64, L_HA = L_OA + 4 * 272, L_W = L_HA + 4 * 144, WSTR = 144,
              L_END = L_W + 192 * WSTR;

#define SB() __builtin_amdgcn_sched_barrier(0)
#define BAR() asm volatile("s_waitcnt lgkmcnt(0)\n\ts_barrier" ::: "memory")

__device__ __forceinline__ float asf(u32 x) { return __builtin_bit_cast(float, x); }
__device__ __forceinline__ u32 asu(float x) { return __builtin_bit_cast(u32, x); }
__device__ __forceinline__ u32 pk_bf16(float lo, float hi) {
  bf16x2 r = {(__bf16)lo, (__bf16)hi};
  return __builtin_bit_cast(u32, r);
}
__device__ __forceinline__ float rbf(float v) { return (float)(__bf16)v; }
template <int CTRL> __device__ __forceinline__ float dpp(float x) {
  return __builtin_bit_cast(float, __builtin_amdgcn_mov_dpp(__builtin_bit_cast(int, x), CTRL, 0xf, 0xf, true));
}
__device__ __forceinline__ float x16_32_max(float x) {
  auto s = __builtin_amdgcn_permlane16_swap(asu(x), asu(x), false, false);
  x = fmaxf(asf(s[0]), asf(s[1]));
  auto t = __builtin_amdgcn_permlane32_swap(asu(x), asu(x), false, false);
  return fmaxf(asf(t[0]), asf(t[1]));
}
__device__ __forceinline__ float x16_32_sum(float x) {
  auto s = __builtin_amdgcn_permlane16_swap(asu(x), asu(x), false, false);
  x = asf(s[0]) + asf(s[1]);
  auto t = __builtin_amdgcn_permlane32_swap(asu(x), asu(x), false, false);
  return asf(t[0]) + asf(t[1]);
}
__device__ __forceinline__ f32x4 mfma(v4u a, v4u b, f32x4 c) {
  return __builtin_amdgcn_mfma_f32_16x16x32_bf16(__builtin_bit_cast(bf16x8, a), __builtin_bit_cast(bf16x8, b), c, 0, 0, 0);
}
template <int I> __device__ __forceinline__ v4u colfrag(const v4u* r) {
  constexpr u32 sel = (I & 1) ? 0x07060302u : 0x05040100u;
  v4u f;
#pragma unroll
  for (int d = 0; d < 4; ++d) f[d] = __builtin_amdgcn_perm(r[2 * d + 1][I >> 1], r[2 * d][I >> 1], sel);
  return f;
}
__device__ __forceinline__ u64 pack2(float a, float b, u32 tag) {
  u32 lo = __builtin_amdgcn_perm(asu(b), asu(a), 0x05030201u), hi = __builtin_amdgcn_perm(tag, asu(b), 0x05040302u);
  return ((u64)hi << 32) | lo;
}
__device__ __forceinline__ float unp_a(u64 g) { return asf((u32)g << 8); }
__device__ __forceinline__ float unp_b(u64 g) { return asf(__builtin_amdgcn_perm((u32)(g >> 32), (u32)g, 0x0504030cu)); }
__device__ __forceinline__ u64 gld(const u64* p) { return __hip_atomic_load((GAS u64*)p, RLX_AGENT); }
__device__ __forceinline__ void gst(u64* p, u64 v) { __hip_atomic_store((GAS u64*)p, v, RLX_AGENT); }
template <int N> __device__ __forceinline__ bool tags_ok(const u64* gr, u32 tagh) {
  u32 bad = 0;
#pragma unroll
  for (int k = 0; k < N; ++k) bad |= (u32)(gr[k] >> 32) ^ tagh;
  return __all(bad < 65536u);
}
__device__ __forceinline__ v4u ldw(const char* p) {
  return __builtin_nontemporal_load((const GAS v4u*)p);
}
#define ST(k) do { SB(); asm volatile("s_memrealtime %0\n\ts_waitcnt lgkmcnt(0)" : "=s"(stamp[k]) :: "memory"); SB(); } while (0)

__global__ void __launch_bounds__(256, 2)
fused_decode(const char* __restrict__ x, const char* __restrict__ kc, const char* __restrict__ vc, const char* __restrict__ wq,
             const char* __restrict__ wo, const char* __restrict__ w1, const char* __restrict__ w3, const char* __restrict__ w2,
             const char* __restrict__ gw, u16* __restrict__ out, u64* __restrict__ ws) {
  __shared__ __attribute__((aligned(16))) char lds[L_END];
  u64 stamp[16];
  u32 tries1 = 0, tries2 = 0, tries3 = 0;
  ST(0);
  const int tid = threadIdx.x, l = tid & 63, w = __builtin_amdgcn_readfirstlane(tid >> 6);
  const int G = blockIdx.x & 7, r = blockIdx.x >> 3, rh = r >> 3, rl = r & 7;
  const int g = l >> 4;
  const bool ffn = G < 7;

  const size_t kvoff = (size_t)((rh * 8 + G) * 8192 + 6144) * 256;
  const __amdgpu_buffer_rsrc_t rk = __builtin_amdgcn_make_buffer_rsrc((void*)(kc + kvoff), 0, 2048 * 256, 0x00020000);
  const __amdgpu_buffer_rsrc_t rv = __builtin_amdgcn_make_buffer_rsrc((void*)(vc + kvoff), 0, 2048 * 256, 0x00020000);
  const u32 koff = ((l >> 2) & 3) * 256 + g * 64 + (l & 3) * 16, voff = l * 16;
  const u32 sbase = (rl * 4 + w) * 1024;
  v4u K[16], V[16];
#define LDK(j) K[j] = __builtin_amdgcn_raw_buffer_load_b128(rk, koff, sbase + (j) * 32768, KV_AUX)
#define LDV(j) V[j] = __builtin_amdgcn_raw_buffer_load_b128(rv, voff, sbase + (j) * 32768, KV_AUX)
  asm volatile("" ::"s"(x), "s"(wq), "s"(w1), "s"(w3), "s"(gw), "s"(ws), "s"(kc));
  LDK(0); LDK(1);
  SB();
  const u32 epoch_v = __hip_atomic_load((GAS u32*)ws, RLX_AGENT);
  const u64 pc = __builtin_amdgcn_s_getpc() & ~127ull;
  const u32 codew = *(const GAS u32*)(pc + (u64)(min(r + 32 * (tid & 1), CODE_LINES - 1) * 128));
  const int krow = 128 * rh + 32 * w + 8 * g;
  const v4u xa = *(const GAS v4u*)(x + (((l & 15) >> 2) * 512 + krow) * 2);
  v4u S1[8];
  {
    const bool m1 = ((l >> 3) & 1) && ffn;
    const char* p0 = wq + (size_t)(krow * 8192 + (8 * G + rl) * 128 + (l & 7) * 16);
    const char* p1 = ((rl >> 2) ? w3 : w1) + (size_t)(krow * 3584 + (4 * G + (rl & 3)) * 128 + (l & 7) * 16);
    const char* p = m1 ? p1 : p0;
    const u32 str = m1 ? 3584u : 8192u;
#pragma unroll
    for (int j = 0; j < 8; ++j) S1[j] = ldw(p + (size_t)(j * str));
  }
  const v4u xg = *(const GAS v4u*)(x + (w * 512 + l * 8) * 2);
  const v4u gv = *(const GAS v4u*)(gw + l * 16);
  SB();
  LDK(2); LDK(3); LDK(4); LDK(5); LDK(6); LDK(7); LDK(8); LDK(9);
  SB();
  ST(1);
  const u32 tag = __builtin_amdgcn_readfirstlane(epoch_v) + 1u;
  const u32 tagh = tag << 16;
  asm volatile("" ::"v"(codew));

  {
    const f32x4 z = {0.f, 0.f, 0.f, 0.f};
    f32x4 d0 = mfma(xa, colfrag<0>(S1), z), d1 = mfma(xa, colfrag<1>(S1), z), d2 = mfma(xa, colfrag<2>(S1), z), d3 = mfma(xa, colfrag<3>(S1), z);
    f32x4 d4 = mfma(xa, colfrag<4>(S1), z), d5 = mfma(xa, colfrag<5>(S1), z), d6 = mfma(xa, colfrag<6>(S1), z), d7 = mfma(xa, colfrag<7>(S1), z);
    float* dst = (float*)(lds + L_RED) + ((w * 2 + ((l >> 3) & 1)) * 4 + g) * 64 + (l & 7) * 8;
    *(f32x4*)dst = f32x4{d0[0], d1[0], d2[0], d3[0]};
    *(f32x4*)(dst + 4) = f32x4{d4[0], d5[0], d6[0], d7[0]};
  }
  BAR();
  {
    const float* src = (const float*)(lds + L_RED) + tid * 2;
    f32x2 a = *(const f32x2*)src, b2 = *(const f32x2*)(src + 512), c = *(const f32x2*)(src + 1024), d = *(const f32x2*)(src + 1536);
    const float s0 = (a[0] + b2[0]) + (c[0] + d[0]), s1 = (a[1] + b2[1]) + (c[1] + d[1]);
    const int bb = (tid >> 5) & 3, pr = tid & 31;
    const u32 iq = WS_Q + (((G * 4 + bb) * 4 + rh) * 8 + rl) * 32 + pr;
    const u32 ih = WS_H + ((((G * 4 + (rl & 3)) * 4 + bb) * 4 + rh) * 2 + (rl >> 2)) * 32 + pr;
    gst(ws + (w < 2 ? iq : ih), pack2(s0, s1, tag));
  }
  SB();
  ST(2);
  LDK(10); LDK(11); LDK(12); LDK(13); LDK(14); LDK(15);
  LDV(0); LDV(1); LDV(2); LDV(3);
  SB();
  float gate;
  {
    float s = 0.f;
#pragma unroll
    for (int d = 0; d < 4; ++d) {
      s += rbf(asf(xg[d] << 16) * asf(gv[d] << 16));
      s += rbf(asf(xg[d] & 0xffff0000u) * asf(gv[d] & 0xffff0000u));
    }
    s += dpp<0xB1>(s); s += dpp<0x4E>(s); s += dpp<0x141>(s); s += dpp<0x128>(s);
    s = rbf(x16_32_sum(s));
    gate = rbf(1.f / (1.f + __expf(-s)));
  }
  SB();
  const u64* qp = ws + WS_Q + (G * 4 + rh) * 1024 + tid;
  u64 gq[4];
#pragma unroll
  for (int k = 0; k < 4; ++k) gq[k] = gld(qp + k * 256);
  SB();
  LDV(4); LDV(5); LDV(6); LDV(7); LDV(8); LDV(9); LDV(10); LDV(11); LDV(12); LDV(13); LDV(14); LDV(15);
  SB();
  ST(3);
  if (!tags_ok<4>(gq, tagh)) {
    for (u32 spins = 0; spins < SPIN_CAP; ++spins) {
      ++tries1;
#pragma unroll
      for (int k = 0; k < 4; ++k) gq[k] = gld(qp + k * 256);
      if (tags_ok<4>(gq, tagh)) break;
    }
  }
  ST(4);
  *(u32*)(lds + L_QS + tid * 4) = pk_bf16((unp_a(gq[0]) + unp_a(gq[1])) + (unp_a(gq[2]) + unp_a(gq[3])), (unp_b(gq[0]) + unp_b(gq[1])) + (unp_b(gq[2]) + unp_b(gq[3])));
  BAR();
  v4u P0, P1;
  float mrow;
  {
    const v4u qb = *(const v4u*)(lds + L_QS + ((l >> 2) & 3) * 256 + (4 * g + (l & 3)) * 16);
    const f32x4 z = {0.f, 0.f, 0.f, 0.f};
    float s[16];
#pragma unroll
    for (int j = 0; j < 16; ++j) {
      const f32x4 d = mfma(K[j], qb, z);
      s[j] = (dpp<0x00>(d[0]) + dpp<0x55>(d[1])) + (dpp<0xAA>(d[2]) + dpp<0xFF>(d[3]));
    }
    float m = s[0];
#pragma unroll
    for (int j = 1; j < 16; ++j) m = fmaxf(m, s[j]);
    m = x16_32_max(m);
    if (l < 16) *(float*)(lds + L_MX + (l >> 2) * 16 + w * 4) = m;
    BAR();
    const f32x4 mm = *(const f32x4*)(lds + L_MX + ((l >> 2) & 3) * 16);
    constexpr float C2 = 0.08838834764831845f * 1.4426950408889634f;
    mrow = fmaxf(fmaxf(mm[0], mm[1]), fmaxf(mm[2], mm[3])) * C2;
    float ls = 0.f;
#pragma unroll
    for (int j = 0; j < 16; ++j) { s[j] = __builtin_amdgcn_exp2f(fmaf(s[j], C2, -mrow)); ls += s[j]; }
    ls = x16_32_sum(ls);
    if (l < 16) *(float*)(lds + L_LS + (l >> 2) * 16 + w * 4) = ls;
    P0 = v4u{pk_bf16(s[0], s[1]), pk_bf16(s[2], s[3]), pk_bf16(s[4], s[5]), pk_bf16(s[6], s[7])};
    P1 = v4u{pk_bf16(s[8], s[9]), pk_bf16(s[10], s[11]), pk_bf16(s[12], s[13]), pk_bf16(s[14], s[15])};
  }
  SB();
  ST(5);
  const int h = 4 * G + rh;
  v4u WO[4], W2[2];
  u64 gh[4];
  const u64* hp = ws + WS_H + ((G * 4 + rh) * 4 + w) * 256 + l;
  {
    const char* p = wo + (size_t)((128 * h + 8 * w + (l >> 3)) * 1024 + rl * 128 + (l & 7) * 16);
#pragma unroll
    for (int j = 0; j < 4; ++j) WO[j] = ldw(p + j * 32768);
    const char* p2 = w2 + (size_t)(ffn ? (64 * h + 8 * w + (l >> 3)) * 1024 + rl * 128 + (l & 7) * 16 : 0);
#pragma unroll
    for (int j = 0; j < 2; ++j) W2[j] = ldw(p2 + j * 32768);
#pragma unroll
    for (int k = 0; k < 4; ++k) gh[k] = gld(hp + k * 64);
  }
  SB();
  BAR();
  {
    const f32x4 lv = *(const f32x4*)(lds + L_LS + w * 16);
    const float v = (l & 1) ? (lv[0] + lv[1]) + (lv[2] + lv[3]) : asf(__builtin_amdgcn_readlane(asu(mrow), 4 * w));
    gst(ws + WS_ML + (((G * 4 + w) * 4 + rh) * 8 + rl) * 2 + (l & 1), ((u64)tag << 32) | asu(v));
  }
  SB();
  ST(6);
  {
    const f32x4 z = {0.f, 0.f, 0.f, 0.f};
    f32x4 d0 = mfma(P0, colfrag<0>(V), z), d1 = mfma(P0, colfrag<1>(V), z), d2 = mfma(P0, colfrag<2>(V), z), d3 = mfma(P0, colfrag<3>(V), z);
    f32x4 d4 = mfma(P0, colfrag<4>(V), z), d5 = mfma(P0, colfrag<5>(V), z), d6 = mfma(P0, colfrag<6>(V), z), d7 = mfma(P0, colfrag<7>(V), z);
    SB();
    d0 = mfma(P1, colfrag<0>(V + 8), d0); d1 = mfma(P1, colfrag<1>(V + 8), d1); d2 = mfma(P1, colfrag<2>(V + 8), d2); d3 = mfma(P1, colfrag<3>(V + 8), d3);
    d4 = mfma(P1, colfrag<4>(V + 8), d4); d5 = mfma(P1, colfrag<5>(V + 8), d5); d6 = mfma(P1, colfrag<6>(V + 8), d6); d7 = mfma(P1, colfrag<7>(V + 8), d7);
    float* dst = (float*)(lds + L_RED) + (w * 4 + g) * 128 + (l & 15) * 8;
    *(f32x4*)dst = f32x4{d0[0], d1[0], d2[0], d3[0]};
    *(f32x4*)(dst + 4) = f32x4{d4[0], d5[0], d6[0], d7[0]};
  }
  ST(7);
  BAR();
  {
    const float* src = (const float*)(lds + L_RED) + tid * 2;
    f32x2 a = *(const f32x2*)src, b2 = *(const f32x2*)(src + 512), c = *(const f32x2*)(src + 1024), d = *(const f32x2*)(src + 1536);
    const float s0 = (a[0] + b2[0]) + (c[0] + d[0]), s1 = (a[1] + b2[1]) + (c[1] + d[1]);
    gst(ws + WS_O + (((G * 4 + w) * 4 + rh) * 8 + rl) * 64 + l, pack2(s0, s1, tag));
  }
  SB();
  ST(8);
  {
    char* dst = lds + L_W + (8 * w + (l >> 3)) * WSTR + (l & 7) * 16;
#pragma unroll
    for (int j = 0; j < 4; ++j) *(v4u*)(dst + j * 32 * WSTR) = WO[j];
    if (ffn) {
#pragma unroll
      for (int j = 0; j < 2; ++j) *(v4u*)(dst + (4 + j) * 32 * WSTR) = W2[j];
      if (!tags_ok<4>(gh, tagh)) {
        for (u32 spins = 0; spins < SPIN_CAP; ++spins) {
#pragma unroll
          for (int k = 0; k < 4; ++k) gh[k] = gld(hp + k * 64);
          if (tags_ok<4>(gh, tagh)) break;
        }
      }
      const float a = rbf((unp_a(gh[0]) + unp_a(gh[1])) + (unp_a(gh[2]) + unp_a(gh[3])));
      const float b2 = rbf((unp_b(gh[0]) + unp_b(gh[1])) + (unp_b(gh[2]) + unp_b(gh[3])));
      auto sa = __builtin_amdgcn_permlane32_swap(asu(a), asu(a), false, false);
      auto sb = __builtin_amdgcn_permlane32_swap(asu(b2), asu(b2), false, false);
      const float ga = asf(sa[0]), ua = asf(sa[1]), gb = asf(sb[0]), ub = asf(sb[1]);
      const float og = 1.f - gate;
      const float ha = rbf(rbf(ga / (1.f + __expf(-ga))) * ua) * og, hb = rbf(rbf(gb / (1.f + __expf(-gb))) * ub) * og;
      if (l < 32) *(u32*)(lds + L_HA + w * 144 + l * 4) = pk_bf16(ha, hb);
    }
  }
  const u64* op = ws + WS_O + ((G * 4 + rh) * 4 + w) * 512 + l;
  const u64* mp = ws + WS_ML + ((G * 4 + rh) * 4 + w) * 16 + (l & 15);
  u64 go[8], gm;
  gm = gld(mp);
#pragma unroll
  for (int k = 0; k < 8; ++k) go[k] = gld(op + k * 64);
  SB();
  BAR();
  v4u BW[4];
  f32x4 acc = {0.f, 0.f, 0.f, 0.f};
  {
    const char* src = lds + L_W + (8 * g + ((l & 15) >> 2)) * WSTR + w * 32 + (l & 3) * 8;
#define TRF(ks) ({ v2u a_ = __builtin_bit_cast(v2u, __builtin_amdgcn_ds_read_tr16_b64_v4i16((LAS s16x4*)(src + (ks) * 32 * WSTR))); \
                   v2u b_ = __builtin_bit_cast(v2u, __builtin_amdgcn_ds_read_tr16_b64_v4i16((LAS s16x4*)(src + ((ks) * 32 + 4) * WSTR))); \
                   v4u{a_[0], a_[1], b_[0], b_[1]}; })
#pragma unroll
    for (int ks = 0; ks < 4; ++ks) BW[ks] = TRF(ks);
    if (ffn) {
      const char* ap = lds + L_HA + ((l & 15) >> 2) * 144 + g * 16;
      acc = mfma(*(const v4u*)ap, TRF(4), acc);
      acc = mfma(*(const v4u*)(ap + 64), TRF(5), acc);
    }
  }
  SB();
  ST(9);
  for (u32 spins = 0;; ++spins) {
    u32 bad = ((u32)(gm >> 32) == tag) ? 0u : ~0u;
#pragma unroll
    for (int k = 0; k < 8; ++k) bad |= (u32)(go[k] >> 32) ^ tagh;
    if (__all(bad < 65536u) || spins >= SPIN_CAP) break;
    ++tries2;
    gm = gld(mp);
#pragma unroll
    for (int k = 0; k < 8; ++k) go[k] = gld(op + k * 64);
  }
  ST(10);
  {
    const float v = asf((u32)gm);
    const float ms = dpp<0xA0>(v), lsv = dpp<0xF5>(v);
    float M = fmaxf(ms, dpp<0x4E>(ms)); M = fmaxf(M, dpp<0x124>(M)); M = fmaxf(M, dpp<0x128>(M));
    const float e = __builtin_amdgcn_exp2f(ms - M);
    float L = e * lsv; L += dpp<0x4E>(L); L += dpp<0x124>(L); L += dpp<0x128>(L);
    const float wt = e * gate / L;
    float o0 = 0.f, o1 = 0.f;
#pragma unroll
    for (int k = 0; k < 8; ++k) {
      const float wk = asf(__builtin_amdgcn_readlane(asu(wt), 2 * k));
      o0 = fmaf(wk, unp_a(go[k]), o0); o1 = fmaf(wk, unp_b(go[k]), o1);
    }
    *(u32*)(lds + L_OA + w * 272 + l * 4) = pk_bf16(o0, o1);
  }
  BAR();
  {
    const char* ap = lds + L_OA + ((l & 15) >> 2) * 272 + g * 16;
#pragma unroll
    for (int ks = 0; ks < 4; ++ks) acc = mfma(*(const v4u*)(ap + ks * 64), BW[ks], acc);
    gst(ws + WS_R + ((rl * 32 + 8 * g + 2 * w + ((l >> 3) & 1)) * 32 + h) * 8 + (l & 7), ((u64)tag << 32) | asu(acc[0]));
  }
  SB();
  ST(11);
  if (w == 0) {
    const u64* rp = ws + WS_R + (rl * 32 + h) * 256 + l;
    u64 gr[4];
    for (u32 spins = 0;; ++spins) {
      bool ok = true;
#pragma unroll
      for (int k = 0; k < 4; ++k) { gr[k] = gld(rp + k * 64); ok &= (u32)(gr[k] >> 32) == tag; }
      if (__all(ok) || spins >= SPIN_CAP) break;
      ++tries3;
    }
    float s = (asf((u32)gr[0]) + asf((u32)gr[1])) + (asf((u32)gr[2]) + asf((u32)gr[3]));
    s += dpp<0x128>(s);
    s = x16_32_sum(s);
    if (l < 8) out[(h >> 3) * 512 + rl * 64 + (h & 7) * 8 + l] = (u16)pk_bf16(s, s);
    if (blockIdx.x == 0 && l == 0) __hip_atomic_store((GAS u32*)ws, tag, RLX_AGENT);
  }
  ST(12);
  if (blockIdx.x == ENC_WG && w == 0) {
    const u64 until = stamp[0] + 3000ull + (stamp[ENC_K] - stamp[0]);
    while (__builtin_amdgcn_s_memrealtime() < until) __builtin_amdgcn_s_sleep(1);
  }
  asm volatile("" :: "s"(stamp[1]), "s"(stamp[2]), "s"(stamp[3]), "s"(stamp[4]), "s"(stamp[5]), "s"(stamp[6]), "s"(stamp[7]), "s"(stamp[8]), "s"(stamp[9]), "s"(stamp[10]), "s"(stamp[11]), "s"(stamp[12]));
}

extern "C" void kernel_launch(void* const* d_in, const int* in_sizes, int n_in, void* d_out, int out_size, void* d_ws, size_t ws_size,
                              hipStream_t stream) {
  fused_decode<<<256, 256, 0, stream>>>((const char*)d_in[0], (const char*)d_in[1], (const char*)d_in[2], (const char*)d_in[3], (const char*)d_in[4],
                                        (const char*)d_in[6], (const char*)d_in[7], (const char*)d_in[8], (const char*)d_in[9], (u16*)d_out, (u64*)d_ws);
}
```

```cpp
#include <hip/hip_runtime.h>
#include <stdint.h>

typedef unsigned u32;
typedef unsigned long long u64;
typedef unsigned short u16;
typedef u32 v4u __attribute__((ext_vector_type(4)));
typedef u32 v2u __attribute__((ext_vector_type(2)));
typedef float f32x4 __attribute__((ext_vector_type(4)));
typedef __bf16 bf16x8 __attribute__((ext_vector_type(8)));
typedef __bf16 bf16x2 __attribute__((ext_vector_type(2)));
typedef short s16x4 __attribute__((ext_vector_type(4)));
#define GAS __attribute__((address_space(1)))
#define LAS __attribute__((address_space(3)))
#define RLX_AGENT __ATOMIC_RELAXED, __HIP_MEMORY_SCOPE_AGENT

#define INFL 40
#define TAU 243
#define T_K 588
#define T36 (T_K + 36 * TAU)
#define WGAP 400
#define TAUW TAU
#define ODELAY 2200
#define RABS 1620
#define RDELAY 13
#define MLEAD 800
#define QPROG 12
#define HPROG 13
#define T_S0 300
#define T_S1 2500
#define DTMAX 28
#define CMAX 200
#define CSL 40
#define CODE_LINES 88

constexpr u32 WS_Q = 64, WS_H = WS_Q + 32768, WS_O = WS_H + 32768, WS_ML = WS_O + 65536, WS_R = WS_ML + 4096, WS_TS = WS_R + 65536;
constexpr u32 SPIN_CAP = 1u << 20, LSPIN_CAP = 1u << 27;
constexpr u32 L_K = 0, L_V = 65536, L_WQ = 114688, L_W = 131072, L_W13 = L_W, L_X = L_W + 16384, L_QS = 155648, QSTR = 272, L_P = L_QS + 4 * QSTR, PSTR = 528,
              L_MX = L_P + 4 * PSTR, L_LS = L_MX + 128, L_OA = L_LS + 128, OSTR = 272, L_HA = L_OA + 4 * OSTR, HSTR = 144, L_GT = L_HA + 4 * HSTR,
              L_WT = L_GT + 16, L_FL = L_WT + 128, L_END = L_FL + 256;
constexpr u32 F_X = L_FL, F_FREE = L_FL + 32, F_Q = L_FL + 64, F_P = L_FL + 128, F_PK = L_FL + 160, F_PV = L_FL + 164, F_W13 = L_FL + 168, F_W = L_FL + 172,
              F_G = L_FL + 176, F_WT = L_FL + 180, F_TEND = L_FL + 184, F_TAG = L_FL + 192, F_H = L_FL + 224, F_DT = L_FL + 240, F_ZERO = L_FL + 244,
              F_DUE = L_FL + 248, F_NIL = L_FL + 252;
constexpr u32 LAG = INFL / 4;
static_assert(INFL >= 16 && INFL % 4 == 0 && INFL <= 56, "progress words");
static_assert(L_END <= 163840, "LDS");

#define SB() __builtin_amdgcn_sched_barrier(0)
#define PIN(x) asm volatile("" : "+v"(x))
#define BAR() asm volatile("s_waitcnt lgkmcnt(0)\n\ts_barrier" ::: "memory")
#define EXIT() asm volatile("s_endpgm" ::: "memory")

__device__ __forceinline__ float asf(u32 x) { return __builtin_bit_cast(float, x); }
__device__ __forceinline__ u32 asu(float x) { return __builtin_bit_cast(u32, x); }
__device__ __forceinline__ u32 pk_bf16(float lo, float hi) {
  bf16x2 r = {(__bf16)lo, (__bf16)hi};
  return __builtin_bit_cast(u32, r);
}
__device__ __forceinline__ float rbf(float v) { return (float)(__bf16)v; }
template <int CTRL> __device__ __forceinline__ float dpp(float x) {
  return __builtin_bit_cast(float, __builtin_amdgcn_mov_dpp(__builtin_bit_cast(int, x), CTRL, 0xf, 0xf, true));
}
__device__ __forceinline__ float x16_32_max(float x) {
  auto s = __builtin_amdgcn_permlane16_swap(asu(x), asu(x), false, false);
  x = fmaxf(asf(s[0]), asf(s[1]));
  auto t = __builtin_amdgcn_permlane32_swap(asu(x), asu(x), false, false);
  return fmaxf(asf(t[0]), asf(t[1]));
}
__device__ __forceinline__ float x16_32_sum(float x) {
  auto s = __builtin_amdgcn_permlane16_swap(asu(x), asu(x), false, false);
  x = asf(s[0]) + asf(s[1]);
  auto t = __builtin_amdgcn_permlane32_swap(asu(x), asu(x), false, false);
  return asf(t[0]) + asf(t[1]);
}
__device__ __forceinline__ float max8(v4u a, v4u b) {
  return fmaxf(fmaxf(fmaxf(asf(a[0]), asf(a[1])), fmaxf(asf(a[2]), asf(a[3]))), fmaxf(fmaxf(asf(b[0]), asf(b[1])), fmaxf(asf(b[2]), asf(b[3]))));
}
__device__ __forceinline__ f32x4 mfma(v4u a, v4u b, f32x4 c) {
  return __builtin_amdgcn_mfma_f32_16x16x32_bf16(__builtin_bit_cast(bf16x8, a), __builtin_bit_cast(bf16x8, b), c, 0, 0, 0);
}
__device__ __forceinline__ v4u lds128(u32 a) { return *(const LAS v4u*)(size_t)a; }
__device__ __forceinline__ v4u lds_tr(u32 a0, u32 a1) {
  v2u x = __builtin_bit_cast(v2u, __builtin_amdgcn_ds_read_tr16_b64_v4i16((LAS s16x4*)(size_t)a0));
  v2u y = __builtin_bit_cast(v2u, __builtin_amdgcn_ds_read_tr16_b64_v4i16((LAS s16x4*)(size_t)a1));
  return v4u{x[0], x[1], y[0], y[1]};
}
__device__ __forceinline__ u64 gran(u32 payload, u32 tag) { return ((u64)tag << 32) | payload; }
__device__ __forceinline__ float unp_a(u64 g) { return asf((u32)g << 16); }
__device__ __forceinline__ float unp_b(u64 g) { return asf((u32)g & 0xffff0000u); }
__device__ __forceinline__ u64 gld(const u64* p) { return __hip_atomic_load((GAS u64*)p, RLX_AGENT); }
__device__ __forceinline__ void gst(u64* p, u64 v) { __hip_atomic_store((GAS u64*)p, v, RLX_AGENT); }
template <int N> __device__ __forceinline__ bool tags_ok(const u64* gr, u32 tag) {
  u32 bad = 0;
#pragma unroll
  for (int k = 0; k < N; ++k) bad |= (u32)(gr[k] >> 32) ^ tag;
  return __all(bad == 0u);
}
template <int N> __device__ __forceinline__ void sweep(u64* gr, const u64* p, int stride, u32 tag) {
#pragma unroll 1
  for (u32 spins = 0; spins < SPIN_CAP; ++spins) {
#pragma unroll
    for (int k = 0; k < N; ++k) gr[k] = gld(p + k * stride);
    if (tags_ok<N>(gr, tag)) break;
  }
}
__device__ __forceinline__ u32 clk16() { return (u32)__builtin_amdgcn_s_memrealtime() * 16u; }
template <int SLEEP> __device__ __forceinline__ void until(u32 t) {
  while ((int)(clk16() - t) < 0)
    if (SLEEP) __builtin_amdgcn_s_sleep(SLEEP);
}
__device__ __forceinline__ v4u mksrd(const char* p, u32 n) {
  const u64 a = (u64)p;
  return v4u{(u32)a, (u32)(a >> 32) & 0xffffu, n, 0x00020000u};
}
__device__ __forceinline__ void dma4(v4u srd, u32 m0, u32 soff, u32 v0, u32 v1, u32 v2, u32 v3) {
  asm volatile("s_mov_b32 m0, %1\n\ts_nop 0\n\tbuffer_load_dwordx4 %3, %0, %2 offen nt lds\n\tbuffer_load_dwordx4 %4, %0, %2 offen offset:1024 nt lds\n\t"
               "buffer_load_dwordx4 %5, %0, %2 offen offset:2048 nt lds\n\tbuffer_load_dwordx4 %6, %0, %2 offen offset:3072 nt lds"
               ::"s"(srd), "s"(m0), "s"(soff), "v"(v0), "v"(v1), "v"(v2), "v"(v3) : "memory");
}
__device__ __forceinline__ void dma4p(v4u srd, u32 m0, u32 v0) {
  asm volatile("s_mov_b32 m0, %1\n\ts_nop 0\n\tbuffer_load_dwordx4 %2, %0, 0 offen lds\n\tbuffer_load_dwordx4 %2, %0, 0 offen offset:1024 lds\n\t"
               "buffer_load_dwordx4 %2, %0, 0 offen offset:2048 lds\n\tbuffer_load_dwordx4 %2, %0, 0 offen offset:3072 lds" ::"s"(srd), "s"(m0), "v"(v0) : "memory");
}
__device__ __forceinline__ void dma1p(v4u srd, u32 m0, u32 v0) {
  asm volatile("s_mov_b32 m0, %1\n\ts_nop 0\n\tbuffer_load_dwordx4 %2, %0, 0 offen lds" ::"s"(srd), "s"(m0), "v"(v0) : "memory");
}
template <int TS> __device__ __forceinline__ void dma_run(v4u srd, u32 m0, u32 m0_end, u32 step, u32 v0, u32 v1, u32 v2, u32 v3, u32 due) {
  u32 c = 0;
  asm volatile("s_mov_b32 m0, %3\nLD%=:\n\ts_memrealtime vcc\n\ts_waitcnt lgkmcnt(0)\n\ts_lshl_b32 vcc_lo, vcc_lo, 4\n\t"
               "s_sub_u32 vcc_lo, vcc_lo, %1\n\ts_cmp_lt_i32 vcc_lo, 0\n\ts_cbranch_scc1 LD%=\n\t"
               "buffer_load_dwordx4 %6, %2, %0 offen nt lds\n\tbuffer_load_dwordx4 %7, %2, %0 offen offset:1024 nt lds\n\t"
               "buffer_load_dwordx4 %8, %2, %0 offen offset:2048 nt lds\n\tbuffer_load_dwordx4 %9, %2, %0 offen offset:3072 nt lds\n\t"
               "s_add_u32 m0, m0, 0x1000\n\ts_add_u32 %0, %0, %5\n\ts_add_u32 %1, %1, %10\n\ts_cmp_lg_u32 m0, %4\n\ts_cbranch_scc1 LD%="
               : "+&s"(c), "+&s"(due)
               : "s"(srd), "s"(m0), "s"(m0_end), "s"(step), "v"(v0), "v"(v1), "v"(v2), "v"(v3), "i"(TS)
               : "memory", "scc", "vcc");
}
__device__ __forceinline__ void dma_kv(v4u srd, u32 m0, u32 x, u32 v0, u32 v1, u32 v2, u32 v3, u32 fprog, u32 fdt, u32 due, u32 tau) {
  u32 soff, c = 0, prog = 0, dt;
  asm volatile("s_mov_b32 m0, %6\nLK%=:\n\ts_memrealtime vcc\n\tds_read_b32 %4, %13\n\ts_xor_b32 %3, %1, %7\n\ts_waitcnt lgkmcnt(0)\n\ts_lshl_b32 vcc_lo, vcc_lo, 4\n\t"
               "s_sub_u32 vcc_lo, vcc_lo, %2\n\ts_cmp_lt_i32 vcc_lo, 0\n\ts_cbranch_scc1 LK%=\n\t"
               "buffer_load_dwordx4 %8, %5, %3 offen nt lds\n\tbuffer_load_dwordx4 %9, %5, %3 offen offset:1024 nt lds\n\t"
               "buffer_load_dwordx4 %10, %5, %3 offen offset:2048 nt lds\n\tbuffer_load_dwordx4 %11, %5, %3 offen offset:3072 nt lds\n\t"
               "v_readfirstlane_b32 vcc_lo, %4\n\ts_add_u32 m0, m0, 0x1000\n\ts_add_u32 %1, %1, 0x9000\n\ts_add_u32 %2, %2, %15\n\tv_add_u32 %0, 1, %0\n\t"
               "s_add_u32 %2, %2, vcc_lo\n\ts_cmp_eq_u32 %1, 0x48000\n\ts_cselect_b32 %1, 0x40000, %1\n\ts_cmp_lg_u32 %1, 0x88000\n\ts_waitcnt vmcnt(%14)\n\t"
               "ds_write_b32 %12, %0\n\ts_cbranch_scc1 LK%=\n\ts_or_b32 %2, %2, 1\n\tv_mov_b32 %4, %2\n\tds_write_b32 %13, %4 offset:8"
               : "+&v"(prog), "+&s"(c), "+&s"(due), "=&s"(soff), "=&v"(dt)
               : "s"(srd), "s"(m0), "s"(x), "v"(v0), "v"(v1), "v"(v2), "v"(v3), "v"(fprog), "v"(fdt), "i"(INFL), "s"(tau)
               : "memory", "scc", "vcc");
}
__device__ __forceinline__ void dma_drain(u32 fprog, u32 p0, u32 p1, u32 p2) {
  asm volatile("s_waitcnt vmcnt(16)\n\tds_write_b32 %0, %1\n\ts_waitcnt vmcnt(8)\n\tds_write_b32 %0, %2\n\ts_waitcnt vmcnt(0)\n\tds_write_b32 %0, %3"
               ::"v"(fprog), "v"(p0), "v"(p1), "v"(p2) : "memory");
}
template <int X> __device__ __forceinline__ void vmwait() { asm volatile("s_waitcnt vmcnt(%0)" ::"i"(X) : "memory"); }
__device__ __forceinline__ void lds_put(u32 addr, u32 v) { asm volatile("ds_write_b32 %0, %1" ::"v"(addr), "v"(v) : "memory"); }
__device__ __forceinline__ void lds_put2(u32 addr, u32 v) { asm volatile("ds_write_b16 %0, %1\n\tds_write_b16_d16_hi %0, %1 offset:32" ::"v"(addr), "v"(v) : "memory"); }
__device__ __forceinline__ u32 lds_get(u32 addr) {
  u32 v;
  asm volatile("ds_read_b32 %0, %1\n\ts_waitcnt lgkmcnt(0)" : "=v"(v) : "v"(addr) : "memory");
  return v;
}
template <int SLEEP> __device__ __forceinline__ void lds_wait(u32 addr, u32 need) {
  u32 v, cap = LSPIN_CAP;
  asm volatile("LW%=:\n\tds_read_b32 %0, %2\n\ts_waitcnt lgkmcnt(0)\n\tv_cmp_le_u32 vcc, %3, %0\n\ts_cmp_eq_u64 vcc, exec\n\ts_cbranch_scc1 LX%=\n\t"
               "s_sleep %4\n\ts_sub_u32 %1, %1, 1\n\ts_cbranch_scc0 LW%=\nLX%=:"
               : "=&v"(v), "+&s"(cap) : "v"(addr), "s"(need), "i"(SLEEP) : "memory", "vcc", "scc");
}

__global__ void __launch_bounds__(768, 3) __attribute__((aligned(4096)))
fused_decode(const char* __restrict__ wq, const char* __restrict__ x, const char* __restrict__ gw, u64* __restrict__ ws, const char* __restrict__ kc,
             const char* __restrict__ w1, const char* __restrict__ w3, const char* __restrict__ vc, const char* __restrict__ wo, const char* __restrict__ w2,
             u16* __restrict__ out) {
  __shared__ __attribute__((aligned(16))) char lds[L_END];
  const int l = threadIdx.x & 63, wv = __builtin_amdgcn_readfirstlane(threadIdx.x >> 6);
  const int G = blockIdx.x & 7, r = blockIdx.x >> 3, rh = r >> 3, rl = r & 7;
  const int g = l >> 4, c4 = (l & 15) >> 2, G2 = G ^ 4, h2 = 4 * G2 + rh, GQ = G2 ^ rh, rf = rl ^ 2;
  asm volatile("" ::"v"((LAS char*)lds));
  u32 codew = 0;
  if (wv == 2) {
    const u64 pc = __builtin_amdgcn_s_getpc() & ~127ull;
    asm volatile("global_load_dword %0, %1, %2" : "=v"(codew) : "v"(min(r + 32 * (l & 3), CODE_LINES - 1) * 128), "s"(pc) : "memory");
  }
  if (wv == 5) lds_put(L_FL + l * 4, 0u);
  const u32 wsw = ((l & 7) ^ ((g & 1) << 1)) << 4;
  u32 q0 = (l >> 3) * 8192 + wsw, q1 = (q0 ^ 64) + 64512, q2 = q0 + 2 * 64512, q3 = (q0 ^ 64) + 3 * 64512, l16 = l * 16;
  u32 qoff = 128 * rh * 8192 + (8 * GQ + rl) * 128;
  asm volatile("" : "+v"(q0), "+v"(q1), "+v"(q2), "+v"(q3), "+v"(l16));
  asm volatile("" : "+s"(qoff));
  SB();
  asm volatile("" ::"s"(x), "s"(kc), "s"(wq), "s"(w1), "s"(w3), "s"(gw), "s"(ws), "s"(vc), "s"(wo), "s"(w2), "s"(out));
  u64 t0q;
  asm volatile("s_memrealtime %0" : "=s"(t0q)::"memory");
  u32 epoch_v = 0;
  if (wv == 2) {
    dma4p(mksrd(x, 4096), L_X, l16);
    dma1p(mksrd(gw, 1024), L_X + 4096, l16);
    asm volatile("global_load_dword %0, %1, %2 sc1" : "=v"(epoch_v) : "v"(0u), "s"(ws) : "memory");
  }
  SB();
  if (wv == 0) {
    const v4u rs = mksrd(wq + qoff, 127u * 8192u + 128u);
    dma4(rs, L_WQ, 0, q0, q1, q2, q3);
    dma4(rs, L_WQ + 4096, 32 * 8192, q0, q1, q2, q3);
    dma4(rs, L_WQ + 8192, 64 * 8192, q0, q1, q2, q3);
    dma4(rs, L_WQ + 12288, 96 * 8192, q0, q1, q2, q3);
  }
  SB();
  asm volatile("s_waitcnt lgkmcnt(0)" : "+s"(t0q)::"memory");
  if (wv == 1 && r == 0) gst(ws + (WS_TS + (l & 7) * 16 + G), t0q);
  SB();
  asm volatile("s_barrier" ::: "memory");
  const u32 t0 = (u32)t0q * 16u;
  auto tend = [&]() -> u32 { const u32 te = __builtin_amdgcn_readfirstlane(lds_get(F_TEND)); return te ? te : t0 + T36; };
  const size_t kvoff = (size_t)((rh * 8 + G) * 8192 + 6144) * 256;
  const u32 vb = g * 256 + (((l & 15) ^ (g << 2)) << 4);
  const u32 v0 = vb, v1 = vb ^ 16, v2 = vb ^ 32, v3 = vb ^ 48;
  if (wv == 3) {
    __builtin_amdgcn_s_setprio(3);
    const v4u rs = mksrd(((rf >> 2) ? w3 : w1) + (size_t)(128 * rh * 3584 + (4 * (G < 7 ? G : 0) + (rf & 3)) * 128), G < 7 ? 127u * 3584u + 128u : 0u);
    const u32 b0 = (l >> 3) * 3584 + wsw, st8 = 8 * 3584 - 1024;
    dma_run<TAU>(rs, L_W13, L_W13 + 16384, 32 * 3584, b0, (b0 ^ 64) + st8, b0 + 2 * st8, (b0 ^ 64) + 3 * st8, t0 + T_K);
  }
  SB();
  if (wv == 1) {
    __builtin_amdgcn_s_setprio(3);
    dma_kv(mksrd(kc + kvoff, 524288), L_K, rl << 12, v0, v1, v2, v3, F_PK, F_DT, t0 + T_K + 4 * TAU, TAU);
    dma_drain(F_PK, LAG + 12, LAG + 14, LAG + 16);
  }
  SB();
  if (wv == 2) {
    asm volatile("s_waitcnt vmcnt(0)" : "+v"(epoch_v), "+v"(codew)::"memory");
    lds_put(F_TAG, epoch_v + 1u);
    lds_put(F_X, 1u);
  }
  SB();
  const u32 tr0 = (8 * g + c4) * 128 + (l & 1) * 8, trx = ((l >> 1) & 1) ^ ((c4 & 2) | ((g & 1) << 2));
  if (wv == 0) {
    lds_wait<1>(F_X, 1u);
    const u32 tag = __builtin_amdgcn_readfirstlane(lds_get(F_TAG));
    u32 wa = L_WQ + tr0 + (trx << 4);
    u64* pub = ws + (WS_Q + (((GQ * 4 + g) * 4 + rh) * 8 + rl) * 32 + (l & 15));
    v4u xf[4];
#pragma unroll
    for (int ks = 0; ks < 4; ++ks) xf[ks] = lds128(L_X + c4 * 1024 + rh * 256 + g * 16 + ks * 64);
#pragma unroll 1
    for (u32 pass = 1; pass < 3; ++pass) {
      u32 wb = wa ^ 32, wc = wa ^ 64, wd = wa ^ 96;
      PIN(wa); PIN(wb); PIN(wc); PIN(wd); PIN(pub);
      f32x4 d0 = {0.f, 0.f, 0.f, 0.f}, d1 = d0, d2 = d0, d3 = d0;
#pragma unroll
      for (int hf = 0; hf < 2; ++hf) {
        if (hf == 0) vmwait<8>(); else vmwait<0>();
        v4u f[8];
#pragma unroll
        for (int k = 0; k < 2; ++k) {
          const u32 o = (hf * 2 + k) * 4096;
          f[4 * k] = lds_tr(wa + o, wa + o + 512); f[4 * k + 1] = lds_tr(wb + o, wb + o + 512);
          f[4 * k + 2] = lds_tr(wc + o, wc + o + 512); f[4 * k + 3] = lds_tr(wd + o, wd + o + 512);
        }
        SB();
#pragma unroll
        for (int k = 0; k < 2; ++k) {
          d0 = mfma(xf[hf * 2 + k], f[4 * k], d0); d1 = mfma(xf[hf * 2 + k], f[4 * k + 1], d1);
          d2 = mfma(xf[hf * 2 + k], f[4 * k + 2], d2); d3 = mfma(xf[hf * 2 + k], f[4 * k + 3], d3);
        }
        SB();
      }
      gst(pub, gran(pk_bf16(d0[0], d1[0]), tag));
      gst(pub + 16, gran(pk_bf16(d2[0], d3[0]), tag));
      lds_put(F_FREE, pass);
      lds_wait<1>(F_W13, 1u);
      wa += L_W13 - L_WQ;
      pub = ws + (WS_H + ((((G * 4 + (rf & 3)) * 4 + g) * 4 + rh) * 2 + (rf >> 2)) * 32 + (l & 15));
    }
    if (blockIdx.x == 3) {
      u64 ge[4];
      lds_wait<4>(F_PV, 16u);
      sweep<4>(ge, ws + (WS_Q + (l >> 5) * 4096 + (l & 31) * 32), 8192, tag);
      __hip_atomic_store((GAS u32*)ws, tag, RLX_AGENT);
    }
    EXIT();
  }
  if (wv == 1) {
    const v4u ro = mksrd(wo + (size_t)h2 * 131072 + rl * 128, 131072 - rl * 128);
    const v4u r2 = mksrd(w2 + (size_t)(G2 < 7 ? h2 : 0) * 65536 + rl * 128, G2 < 7 ? 65536 - rl * 128 : 0);
    const u32 a0 = (l >> 3) * 1024 + wsw, a1 = (a0 ^ 64) + 7168, a2 = a0 + 2 * 7168, a3 = (a0 ^ 64) + 3 * 7168;
    const u32 tw = tend() + WGAP;
    lds_wait<1>(F_FREE, 2u);
    lds_wait<1>(F_G, 1u);
    lds_wait<1>(F_PV, 16u);
    dma_run<TAUW>(r2, L_W + 16384, L_W + 24576, 32768, a0, a1, a2, a3, tw);
    dma_run<TAUW>(ro, L_W, L_W + 16384, 32768, a0, a1, a2, a3, tw + 2 * TAUW);
    dma_drain(F_W, 1u, 2u, 3u);
    EXIT();
  }
  if (wv == 3) {
    vmwait<0>();
    lds_put(F_W13, 1u);
    lds_wait<4>(F_PK, 14u);
    lds_wait<1>(F_FREE, 1u);
    const u32 te = tend();
    lds_wait<0>(F_DUE, 1u);
    const u32 due = __builtin_amdgcn_readfirstlane(lds_get(F_DUE));
    dma_kv(mksrd(vc + kvoff, 524288), L_V, rl << 12, v0, v1, v2, v3, F_PV, F_ZERO, due, max((int)(te - due) >> 4, TAU - DTMAX));
    dma_drain(F_PV, LAG + 12, LAG + 14, LAG + 16);
    EXIT();
  }
  if (wv == 2) {
    const u32 tag = __builtin_amdgcn_readfirstlane(epoch_v) + 1u;
    float gate;
    {
      float s = 0.f;
#pragma unroll 1
      for (int k = 0; k < 4; ++k) {
        const v4u a = lds128(L_X + g * 1024 + k * 256 + (l & 15) * 16), b = lds128(L_X + 4096 + k * 256 + (l & 15) * 16);
#pragma unroll
        for (int d = 0; d < 4; ++d) {
          const u32 pr = pk_bf16(asf(a[d] << 16) * asf(b[d] << 16), asf(a[d] & 0xffff0000u) * asf(b[d] & 0xffff0000u));
          s += asf(pr << 16) + asf(pr & 0xffff0000u);
        }
      }
      s += dpp<0xB1>(s); s += dpp<0x4E>(s); s += dpp<0x141>(s); s += dpp<0x128>(s);
      gate = rbf(__builtin_amdgcn_rcpf(1.f + __expf(-rbf(s))));
      lds_put(L_GT + g * 4, asu(gate));
      lds_put(F_G, 1u);
    }
    u64* mpub = ws + (WS_ML + ((G * 4 + rh) * 8 + rl) * 16 + g * 2 + (l & 1));
    const u64* mp = ws + (WS_ML + ((G2 * 4 + g) * 8 + ((l & 15) >> 1)) * 16 + rh * 2 + (l & 1));
    PIN(mpub); PIN(mp);
    lds_wait<1>(F_P + (l & 7) * 4, 1u);
    {
      const v4u ma = lds128(L_MX + g * 32), mb = lds128(L_MX + g * 32 + 16), la = lds128(L_LS + g * 32), lb = lds128(L_LS + g * 32 + 16);
      const float M = max8(ma, mb);
      float lsum = 0.f;
#pragma unroll
      for (int k = 0; k < 4; ++k) lsum = fmaf(__builtin_amdgcn_exp2f(asf(ma[k]) - M), asf(la[k]), fmaf(__builtin_amdgcn_exp2f(asf(mb[k]) - M), asf(lb[k]), lsum));
      gst(mpub, gran(asu((l & 1) ? lsum : M), tag));
    }
    const u32 tm = tend() + ODELAY - MLEAD;
    lds_wait<1>(F_PV, 16u);
    u64 gm;
#pragma unroll 1
    for (u32 spins = 0; spins < SPIN_CAP; ++spins) {
      gm = gld(mp);
      if (tags_ok<1>(&gm, tag)) break;
      until<1>(tm);
    }
    const float v = asf((u32)gm);
    const float ms = dpp<0xA0>(v), lsv = dpp<0xF5>(v);
    float M = fmaxf(ms, dpp<0x4E>(ms)); M = fmaxf(M, dpp<0x124>(M)); M = fmaxf(M, dpp<0x128>(M));
    const float e = __builtin_amdgcn_exp2f(ms - M);
    float L = e * lsv; L += dpp<0x4E>(L); L += dpp<0x124>(L); L += dpp<0x128>(L);
    lds_put(L_WT + (l >> 1) * 4, asu(e * gate * __builtin_amdgcn_rcpf(L)));
    lds_put(F_WT, 1u);
    EXIT();
  }
  const int w = wv - 4, ct = w & 3;
  if (w == 7) {
    float slot = 0.f, tau = TAU, tprev = T_K;
#pragma unroll 1
    for (int pass = 0; pass < 2; ++pass) {
      until<1>(t0 + (pass ? T_S1 : T_S0));
      const u64 now = __builtin_amdgcn_s_memrealtime();
      const u64 tj = gld(ws + (WS_TS + G * 16 + (l & 7)));
      const bool ok = now - tj < 400ull;
      float d = 16.f * (float)(int)(u32)((ok ? tj : pass ? now - 40ull : t0q) - t0q), dm = d;
      d += dpp<0xB1>(d); d += dpp<0x4E>(d); d += dpp<0x141>(d);
      dm = fmaxf(dm, dpp<0xB1>(dm)); dm = fmaxf(dm, dpp<0x4E>(dm)); dm = fmaxf(dm, dpp<0x141>(dm));
      const float te = fminf(fmaxf(0.125f * d, dm - fminf(CMAX + (CSL * 0.01f) * (dm - 0.125f * d), DTMAX * 28)), 4000.f) + T36;
      const float tn = fmaxf((float)(int)(clk16() - t0), T_K + 4 * TAU);
      slot += (tn - tprev) * __builtin_amdgcn_rcpf(tau);
      tprev = tn;
      tau = fminf(fmaxf((te - tn) * __builtin_amdgcn_rcpf(36.f - slot), TAU - DTMAX), 2 * TAU);
      lds_put(F_DT, (u32)((int)tau - TAU));
      lds_put(F_TEND, (t0 + (u32)(int)te) | 1u);
      if (__all(ok)) break;
    }
  }
  lds_wait<1>(F_X, 1u);
  const u32 tag = __builtin_amdgcn_readfirstlane(lds_get(F_TAG));
  u64* pub = ws + ((w < 4 ? WS_O + (((G * 4 + g) * 4 + rh) * 8 + rl) * 64 : WS_R + ((rl * 32 + h2) * 4 + g) * 64) + ct * 16 + (l & 15));
  const u32 p2 = g * 64 + (l & 15) * 2;
  u64 gq[4];
  {
    const u64* gp = ws + (WS_Q + (G * 4 + rh) * 1024 + ct * 64 + l);
    if (w < 4) {
      lds_wait<1>(F_PK, QPROG);
#pragma unroll
      for (int k = 0; k < 4; ++k) gq[k] = gld(gp + k * 256);
    }
    SB();
    u32 qa = L_QS + c4 * QSTR + g * 16;
    const u32 ka = L_K + w * 8192 + (l & 15) * 256 + ((g ^ c4 ^ ((l & 3) << 2)) << 4);
    v4u kf[8];
    lds_wait<1>(F_PK, LAG + 2 * w + 2);
#pragma unroll
    for (int ks = 0; ks < 4; ++ks) { kf[2 * ks] = lds128(ka ^ (ks << 6)); kf[2 * ks + 1] = lds128((ka ^ (ks << 6)) + 4096); }
#pragma unroll
    for (int k = 0; k < 8; ++k) PIN(kf[k]);
    u32 amw = L_MX + c4 * 32 + w * 4, afw = F_P + w * 4, apw = L_P + c4 * PSTR + w * 64 + (l & 1) * 32 + g * 8;
    PIN(qa); PIN(amw); PIN(afw); PIN(apw);
    if (w < 4) {
      if (!tags_ok<4>(gq, tag)) sweep<4>(gq, gp, 256, tag);
      lds_put2(L_QS + w * QSTR + p2,
               pk_bf16((unp_a(gq[0]) + unp_a(gq[1])) + (unp_a(gq[2]) + unp_a(gq[3])), (unp_b(gq[0]) + unp_b(gq[1])) + (unp_b(gq[2]) + unp_b(gq[3]))));
      lds_put(F_Q + w * 4, 1u);
    }
    lds_wait<1>(F_Q + (l & 3) * 4, 1u);
    v4u qf[4];
#pragma unroll
    for (int ks = 0; ks < 4; ++ks) qf[ks] = lds128(qa + ks * 64);
    SB();
    f32x4 s0 = {0.f, 0.f, 0.f, 0.f}, s1 = {0.f, 0.f, 0.f, 0.f};
#pragma unroll
    for (int ks = 0; ks < 4; ++ks) { s0 = mfma(kf[2 * ks], qf[ks], s0); s1 = mfma(kf[2 * ks + 1], qf[ks], s1); }
    constexpr float C2 = 0.08838834764831845f * 1.4426950408889634f;
    float m = fmaxf(fmaxf(fmaxf(s0[0], s0[1]), fmaxf(s0[2], s0[3])), fmaxf(fmaxf(s1[0], s1[1]), fmaxf(s1[2], s1[3])));
    m = x16_32_max(m) * C2;
    const bool t1 = l & 1;
    float p[4], ls = 0.f;
#pragma unroll
    for (int i = 0; i < 4; ++i) { p[i] = __builtin_amdgcn_exp2f(fmaf(t1 ? s1[i] : s0[i], C2, -m)); ls += p[i]; }
    *(LAS v2u*)(size_t)apw = v2u{pk_bf16(p[0], p[1]), pk_bf16(p[2], p[3])};
    lds_put(amw, asu(m));
    ls += dpp<0xB1>(ls);
    ls = x16_32_sum(ls);
    lds_put(amw + 128, asu(ls));
    lds_put(afw, 1u);
  }
  const u32 tsweep = tend() + ODELAY;
  if (w < 4) {
    const u64* op = ws + (WS_O + (h2 * 4 + w) * 512 + l);
    u32 aow = L_OA + w * OSTR + p2;
    const u64* rp = ws + (WS_R + ((rl * 32 + (l >> 3)) * 4 + (h2 >> 3)) * 64 + (h2 & 7) * 8 + (l & 7));
    u16* outp = out + ((h2 >> 3) * 512 + rl * 64 + (h2 & 7) * 8 + (l & 7));
    PIN(op); PIN(aow); PIN(rp); PIN(outp);
    lds_wait<0>(F_P + (l & 7) * 4, 1u);
    {
      const v4u ma = lds128(L_MX + g * 32), mb = lds128(L_MX + g * 32 + 16);
      const float M = max8(ma, mb);
      float cf[8];
#pragma unroll
      for (int k = 0; k < 4; ++k) { cf[k] = __builtin_amdgcn_exp2f(asf(ma[k]) - M); cf[4 + k] = __builtin_amdgcn_exp2f(asf(mb[k]) - M); }
      v4u pf[8];
#pragma unroll
      for (int ks = 0; ks < 8; ++ks) pf[ks] = lds128(L_P + c4 * PSTR + ks * 64 + g * 16);
      const u32 ch = 4 * w + ((l >> 1) & 1);
      u32 va0 = L_V + (8 * g + c4) * 256 + ((ch ^ ((c4 << 2) | ((2 * g) & 3))) << 4) + (l & 1) * 8;
      u32 va1 = (va0 ^ 16) + 1024, vb0 = va0 ^ 32, vb1 = va1 ^ 32;
      PIN(va0); PIN(va1); PIN(vb0); PIN(vb1);
#pragma unroll
      for (int k = 0; k < 8; ++k) PIN(cf[k]);
      const f32x4 z = {0.f, 0.f, 0.f, 0.f};
      float o0 = 0.f, o1 = 0.f;
      lds_wait<1>(F_PV, LAG + 12);
      {
        v4u f[12];
#pragma unroll
        for (int ks = 0; ks < 6; ++ks) { f[2 * ks] = lds_tr(va0 + ks * 8192, va1 + ks * 8192); f[2 * ks + 1] = lds_tr(vb0 + ks * 8192, vb1 + ks * 8192); }
        SB();
        f32x4 d[12];
#pragma unroll
        for (int ks = 0; ks < 6; ++ks) { d[2 * ks] = mfma(pf[ks], f[2 * ks], z); d[2 * ks + 1] = mfma(pf[ks], f[2 * ks + 1], z); }
        SB();
#pragma unroll
        for (int ks = 0; ks < 6; ++ks) { o0 = fmaf(cf[ks], d[2 * ks][0], o0); o1 = fmaf(cf[ks], d[2 * ks + 1][0], o1); }
        PIN(o0); PIN(o1);
      }
#pragma unroll
      for (int ks = 6; ks < 8; ++ks) {
        lds_wait<0>(F_PV, LAG + 2 * ks + 2);
        const v4u fa = lds_tr(va0 + ks * 8192, va1 + ks * 8192), fb = lds_tr(vb0 + ks * 8192, vb1 + ks * 8192);
        SB();
        const f32x4 da = mfma(pf[ks], fa, z), db = mfma(pf[ks], fb, z);
        o0 = fmaf(cf[ks], da[0], o0); o1 = fmaf(cf[ks], db[0], o1);
        PIN(o0); PIN(o1);
      }
      gst(pub, gran(pk_bf16(o0, o1), tag));
      SB();
    }
    {
      u64 go[8];
      until<1>(tsweep);
#pragma unroll
      for (int k = 0; k < 8; ++k) go[k] = gld(op + k * 64);
      SB();
      lds_wait<0>(F_WT, 1u);
      v4u wk0 = lds128(L_WT + w * 32), wk1 = lds128(L_WT + w * 32 + 16);
      asm volatile("" : "+v"(wk0), "+v"(wk1));
      SB();
      auto combine = [&]() -> u32 {
        float o0 = asf(wk0[0]) * unp_a(go[0]), o1 = asf(wk0[0]) * unp_b(go[0]);
#pragma unroll
        for (int k = 1; k < 4; ++k) { o0 = fmaf(asf(wk0[k]), unp_a(go[k]), o0); o1 = fmaf(asf(wk0[k]), unp_b(go[k]), o1); }
#pragma unroll
        for (int k = 0; k < 4; ++k) { o0 = fmaf(asf(wk1[k]), unp_a(go[4 + k]), o0); o1 = fmaf(asf(wk1[k]), unp_b(go[4 + k]), o1); }
        return pk_bf16(o0, o1);
      };
      lds_put2(aow, combine());
      if (!tags_ok<8>(go, tag)) {
        sweep<8>(go, op, 64, tag);
        lds_put2(aow, combine());
      }
      BAR();
    }
    if (w == 0) {
      u64 gr[4];
      __builtin_amdgcn_s_sleep(RDELAY);
      until<0>(tsweep + RABS);
      sweep<4>(gr, rp, 2048, tag);
      float s = (asf((u32)gr[0]) + asf((u32)gr[1])) + (asf((u32)gr[2]) + asf((u32)gr[3]));
      s += dpp<0x128>(s);
      s = x16_32_sum(s);
      if (l < 8) *(GAS u16*)outp = (u16)pk_bf16(s, s);
    }
  } else {
    lds_wait<1>(F_PV, HPROG);
    sweep<4>(gq, ws + (WS_H + (h2 * 4 + ct) * 256 + l), 64, tag);
    const float a = rbf((unp_a(gq[0]) + unp_a(gq[1])) + (unp_a(gq[2]) + unp_a(gq[3]))), b2 = rbf((unp_b(gq[0]) + unp_b(gq[1])) + (unp_b(gq[2]) + unp_b(gq[3])));
    auto sa = __builtin_amdgcn_permlane32_swap(asu(a), asu(a), false, false);
    auto sb = __builtin_amdgcn_permlane32_swap(asu(b2), asu(b2), false, false);
    const float ga = asf(sa[0]), ua = asf(sa[1]), gb = asf(sb[0]), ub = asf(sb[1]);
    lds_wait<0>(F_G, 1u);
    const float og = 1.f - asf(lds_get(L_GT + ct * 4));
    const float ha = rbf(rbf(ga * __builtin_amdgcn_rcpf(1.f + __expf(-ga))) * ua) * og, hb = rbf(rbf(gb * __builtin_amdgcn_rcpf(1.f + __expf(-gb))) * ub) * og;
    lds_put2(L_HA + ct * HSTR + (p2 & 127), pk_bf16(ha, hb));
    lds_put(F_H + ct * 4, 1u);
    lds_wait<0>(F_H + (l & 3) * 4, 1u);
    const v4u h0 = lds128(L_HA + c4 * HSTR + g * 16), h1 = lds128(L_HA + c4 * HSTR + g * 16 + 64);
    u32 wa = L_W + tr0 + ((trx ^ (2 * ct)) << 4), oa = L_OA + c4 * OSTR + g * 16;
    PIN(wa); PIN(oa);
    f32x4 acc = {0.f, 0.f, 0.f, 0.f};
    v4u bw[4];
    lds_wait<1>(F_W, 1u);
    if (G2 < 7) {
      acc = mfma(h0, lds_tr(wa + 16384, wa + 16384 + 512), acc);
      acc = mfma(h1, lds_tr(wa + 20480, wa + 20480 + 512), acc);
    }
    lds_wait<0>(F_W, 2u);
    bw[0] = lds_tr(wa, wa + 512); bw[1] = lds_tr(wa + 4096, wa + 4096 + 512);
    lds_wait<0>(F_W, 3u);
    bw[2] = lds_tr(wa + 8192, wa + 8192 + 512); bw[3] = lds_tr(wa + 12288, wa + 12288 + 512);
    BAR();
    v4u of[4];
#pragma unroll
    for (int ks = 0; ks < 4; ++ks) of[ks] = lds128(oa + ks * 64);
    SB();
    f32x4 ac2 = mfma(of[1], bw[1], f32x4{0.f, 0.f, 0.f, 0.f});
    acc = mfma(of[0], bw[0], acc);
    ac2 = mfma(of[3], bw[3], ac2);
    acc = mfma(of[2], bw[2], acc);
    gst(pub, gran(asu(acc[0] + ac2[0]), tag));
  }
}

extern "C" void kernel_launch(void* const* d_in, const int* in_sizes, int n_in, void* d_out, int out_size, void* d_ws, size_t ws_size,
                              hipStream_t stream) {
  fused_decode<<<256, 768, 0, stream>>>((const char*)d_in[3], (const char*)d_in[0], (const char*)d_in[9], (u64*)d_ws, (const char*)d_in[1], (const char*)d_in[6],
                                        (const char*)d_in[7], (const char*)d_in[2], (const char*)d_in[4], (const char*)d_in[8], (u16*)d_out);
}
```

```cpp
#include <hip/hip_runtime.h>
#include <stdint.h>

typedef unsigned u32;
typedef unsigned long long u64;
typedef unsigned short u16;
typedef u32 v4u __attribute__((ext_vector_type(4)));
typedef u32 v2u __attribute__((ext_vector_type(2)));
typedef float f32x4 __attribute__((ext_vector_type(4)));
typedef __bf16 bf16x8 __attribute__((ext_vector_type(8)));
typedef __bf16 bf16x2 __attribute__((ext_vector_type(2)));
typedef short s16x4 __attribute__((ext_vector_type(4)));
#define GAS __attribute__((address_space(1)))
#define LAS __attribute__((address_space(3)))
#define RLX_AGENT __ATOMIC_RELAXED, __HIP_MEMORY_SCOPE_AGENT

#define INFL 40
#define TAU 228
#define T_K 600
#define TAU13 250
#define T4 (T_K + 4 * TAU13)
#define T36 (T4 + 32 * TAU)
#define WGAP 400
#define TAUW 242
#define ODELAY 2080
#define RABS 1640
#define RDELAY 570
#define MLEAD 800
#define QPROG 11
#define HPROG 12
#define T_S0 1400
#define T_S1 2700
#define DTMAX 28
#define CMAX 200
#define CSL 48
#define VG(i) (u32[]){0x21b, 0x5c, 0x3c4, 0x487}[i]
#define VN(i) (u32[]){0x345, 0x6dd, 0xf4, 0x7f}[i]
#define VR(i) (u32[]){0x323, 0x73, 0x331}[i]
#define VP(i) (u32[]){0x120, 0x586, 0x1e4, 0x26a, 0x19b}[i]
#define WG(i) (u32[]){0x4, 0x2, 0x8, 0x1}[i]
#define WO(i) (u32[]){0x0, 0x0, 0x8, 0x1, 0x0, 0x0, 0x0, 0x0}[i]
#define UG(i) (u32[]){0x4, 0x2, 0x1}[i]
#define UO(i) (u32[]){0x0, 0x0, 0x0, 0x1, 0x0, 0x0, 0x0, 0x0}[i]
#define XO(i) (u32[]){0x0, 0x1, 0x0, 0x2, 0x0, 0x0, 0x0, 0x0}[i]
#define QO(i) (u32[]){0x1, 0x2, 0x0, 0x0, 0x0}[i]
#define CODE_LINES 94

constexpr u32 WS_Q = 512, WS_H = WS_Q + 32768, WS_O = WS_H + 32768, WS_ML = WS_O + 65536, WS_R = WS_ML + 4096, WS_TS = WS_R + 65536;
constexpr u32 SPIN_CAP = 1u << 20, LSPIN_CAP = 1u << 27;
constexpr u32 L_K = 0, L_V = 65536, L_WQ = 114688, L_W = 131072, L_W13 = L_W, L_X = L_W + 16384, L_QS = 155648, QSTR = 272, L_P = L_QS + 4 * QSTR, PSTR = 528,
              L_MX = L_P + 4 * PSTR, L_LS = L_MX + 128, L_OA = L_LS + 128, OSTR = 272, L_HA = L_OA + 4 * OSTR, HSTR = 144, L_GT = L_HA + 4 * HSTR,
              L_WT = L_GT + 16, L_FL = L_WT + 128, L_END = L_FL + 256;
constexpr u32 F_X = L_FL, F_FREE = L_FL + 32, F_Q = L_FL + 64, F_P = L_FL + 128, F_PK = L_FL + 160, F_PV = L_FL + 164, F_W13 = L_FL + 168, F_W = L_FL + 172,
              F_G = L_FL + 176, F_WT = L_FL + 180, F_TEND = L_FL + 184, F_TAG = L_FL + 192, F_H = L_FL + 224, F_DT = L_FL + 240, F_ZERO = L_FL + 244,
              F_DUE = L_FL + 248, F_NIL = L_FL + 252;
constexpr u32 LAG = INFL / 4;
static_assert(INFL >= 24 && INFL <= 40 && INFL % 4 == 0 && INFL <= 56, "progress words");
static_assert(L_END <= 163840, "LDS");

template <int V> struct IC { static constexpr int value = V; };
#define SB() __builtin_amdgcn_sched_barrier(0)
#define PIN(x) asm volatile("" : "+v"(x))
#define BAR() asm volatile("s_waitcnt lgkmcnt(0)\n\ts_barrier" ::: "memory")
#define EXIT() asm volatile("s_endpgm" ::: "memory")

__device__ __forceinline__ float asf(u32 x) { return __builtin_bit_cast(float, x); }
__device__ __forceinline__ u32 asu(float x) { return __builtin_bit_cast(u32, x); }
__device__ __forceinline__ u32 pk_bf16(float lo, float hi) {
  bf16x2 r = {(__bf16)lo, (__bf16)hi};
  return __builtin_bit_cast(u32, r);
}
__device__ __forceinline__ float rbf(float v) { return (float)(__bf16)v; }
template <int CTRL> __device__ __forceinline__ float dpp(float x) {
  return __builtin_bit_cast(float, __builtin_amdgcn_mov_dpp(__builtin_bit_cast(int, x), CTRL, 0xf, 0xf, true));
}
__device__ __forceinline__ float x16_32_max(float x) {
  auto s = __builtin_amdgcn_permlane16_swap(asu(x), asu(x), false, false);
  x = fmaxf(asf(s[0]), asf(s[1]));
  auto t = __builtin_amdgcn_permlane32_swap(asu(x), asu(x), false, false);
  return fmaxf(asf(t[0]), asf(t[1]));
}
__device__ __forceinline__ float x16_32_sum(float x) {
  auto s = __builtin_amdgcn_permlane16_swap(asu(x), asu(x), false, false);
  x = asf(s[0]) + asf(s[1]);
  auto t = __builtin_amdgcn_permlane32_swap(asu(x), asu(x), false, false);
  return asf(t[0]) + asf(t[1]);
}
__device__ __forceinline__ float max8(v4u a, v4u b) {
  return fmaxf(fmaxf(fmaxf(asf(a[0]), asf(a[1])), fmaxf(asf(a[2]), asf(a[3]))), fmaxf(fmaxf(asf(b[0]), asf(b[1])), fmaxf(asf(b[2]), asf(b[3]))));
}
__device__ __forceinline__ float expd(float a, float b) { return __builtin_amdgcn_exp2f((a - b) * (0.08838834764831845f * 1.4426950408889634f)); }
__device__ __forceinline__ f32x4 mfma(v4u a, v4u b, f32x4 c) {
  return __builtin_amdgcn_mfma_f32_16x16x32_bf16(__builtin_bit_cast(bf16x8, a), __builtin_bit_cast(bf16x8, b), c, 0, 0, 0);
}
__device__ __forceinline__ v4u lds128(u32 a) { return *(const LAS v4u*)(size_t)a; }
__device__ __forceinline__ v4u lds_tr(u32 a0, u32 a1) {
  v2u x = __builtin_bit_cast(v2u, __builtin_amdgcn_ds_read_tr16_b64_v4i16((LAS s16x4*)(size_t)a0));
  v2u y = __builtin_bit_cast(v2u, __builtin_amdgcn_ds_read_tr16_b64_v4i16((LAS s16x4*)(size_t)a1));
  return v4u{x[0], x[1], y[0], y[1]};
}
__device__ __forceinline__ u64 gran(u32 payload, u32 tag) { return ((u64)tag << 32) | payload; }
__device__ __forceinline__ u64 gld(const u64* p) { return __hip_atomic_load((GAS u64*)p, RLX_AGENT); }
__device__ __forceinline__ void gst(u64* p, u64 v) { __hip_atomic_store((GAS u64*)p, v, RLX_AGENT); }
template <int N> __device__ __forceinline__ bool tags_ok(const u64* gr, u32 tag) {
  u32 bad = 0;
#pragma unroll
  for (int k = 0; k < N; ++k) bad |= (u32)(gr[k] >> 32) ^ tag;
  return __all(bad == 0u);
}
template <int N> __device__ __forceinline__ void sweep(u64* gr, const u64* p, int stride, u32 tag) {
#pragma unroll 1
  for (u32 spins = 0; spins < SPIN_CAP; ++spins) {
#pragma unroll
    for (int k = 0; k < N; ++k) gr[k] = gld(p + k * stride);
    if (tags_ok<N>(gr, tag)) break;
  }
}
template <int N> __device__ __forceinline__ void fetch2(v4u* gr, __amdgpu_buffer_rsrc_t rs, u32 off, int stride) {
#pragma unroll
  for (int k = 0; k < N; ++k) gr[k] = __builtin_amdgcn_raw_buffer_load_b128(rs, off, k * stride, 16  );
  asm volatile("" ::: "memory");
}
template <int N> __device__ __forceinline__ bool fits2(const v4u* gr, u32 tag) {
  u32 bad = 0;
#pragma unroll
  for (int k = 0; k < N; ++k) bad |= (gr[k][1] ^ tag) | (gr[k][3] ^ tag);
  return __all(bad == 0u);
}
template <int N> __device__ __forceinline__ void sweep2(v4u* gr, __amdgpu_buffer_rsrc_t rs, u32 off, int stride, u32 tag) {
#pragma unroll 1
  for (u32 spins = 0; spins < SPIN_CAP; ++spins) {
    fetch2<N>(gr, rs, off, stride);
    if (fits2<N>(gr, tag)) break;
  }
}
__device__ __forceinline__ float halves(float a, float b) {
  auto s = __builtin_amdgcn_permlane32_swap(asu(a), asu(b), false, false);
  return asf(s[0]) + asf(s[1]);
}
__device__ __forceinline__ u32 clk16() { return (u32)__builtin_amdgcn_s_memrealtime() * 16u; }
template <int SLEEP> __device__ __forceinline__ void until(u32 t) {
  while ((int)(clk16() - t) < 0)
    if (SLEEP) __builtin_amdgcn_s_sleep(SLEEP);
}
__device__ __forceinline__ v4u mksrd(const char* p, u32 n) {
  const u64 a = (u64)p;
  return v4u{(u32)a, (u32)(a >> 32) & 0xffffu, n, 0x00020000u};
}
__device__ __forceinline__ void dma4(v4u srd, u32 m0, u32 soff, u32 v0, u32 v1, u32 v2, u32 v3) {
  asm volatile("s_mov_b32 m0, %1\n\ts_nop 0\n\tbuffer_load_dwordx4 %3, %0, %2 offen nt lds\n\tbuffer_load_dwordx4 %4, %0, %2 offen offset:1024 nt lds\n\t"
               "buffer_load_dwordx4 %5, %0, %2 offen offset:2048 nt lds\n\tbuffer_load_dwordx4 %6, %0, %2 offen offset:3072 nt lds"
               ::"s"(srd), "s"(m0), "s"(soff), "v"(v0), "v"(v1), "v"(v2), "v"(v3) : "memory");
}
__device__ __forceinline__ void dma4p(v4u srd, u32 m0, u32 v0) {
  asm volatile("s_mov_b32 m0, %1\n\ts_nop 0\n\tbuffer_load_dwordx4 %2, %0, 0 offen lds\n\tbuffer_load_dwordx4 %2, %0, 0 offen offset:1024 lds\n\t"
               "buffer_load_dwordx4 %2, %0, 0 offen offset:2048 lds\n\tbuffer_load_dwordx4 %2, %0, 0 offen offset:3072 lds" ::"s"(srd), "s"(m0), "v"(v0) : "memory");
}
__device__ __forceinline__ void dma1p(v4u srd, u32 m0, u32 v0) {
  asm volatile("s_mov_b32 m0, %1\n\ts_nop 0\n\tbuffer_load_dwordx4 %2, %0, 0 offen lds" ::"s"(srd), "s"(m0), "v"(v0) : "memory");
}
template <int N> __device__ __forceinline__ void dma_run(v4u srd, u32 m0, u32 x, u32 step, u32 v0, u32 v1, u32 v2, u32 v3, u32 due, u32 ts) {
  u32 n = 0, so;
  asm volatile("LD%=:\n\ts_memrealtime vcc\n\ts_xor_b32 %2, %0, %5\n\ts_lshl_b32 m0, %2, 12\n\ts_add_u32 m0, m0, %4\n\ts_mul_i32 %2, %2, %6\n\t"
               "s_waitcnt lgkmcnt(0)\n\ts_lshl_b32 vcc_lo, vcc_lo, 4\n\t"
               "s_sub_u32 vcc_lo, vcc_lo, %1\n\ts_cmp_lt_i32 vcc_lo, 0\n\ts_cbranch_scc1 LD%=\n\t"
               "buffer_load_dwordx4 %7, %3, %2 offen nt lds\n\tbuffer_load_dwordx4 %8, %3, %2 offen offset:1024 nt lds\n\t"
               "buffer_load_dwordx4 %9, %3, %2 offen offset:2048 nt lds\n\tbuffer_load_dwordx4 %10, %3, %2 offen offset:3072 nt lds\n\t"
               "s_add_u32 %0, %0, 1\n\ts_add_u32 %1, %1, %11\n\ts_cmp_lg_u32 %0, %12\n\ts_cbranch_scc1 LD%="
               : "+&s"(n), "+&s"(due), "=&s"(so)
               : "s"(srd), "s"(m0), "s"(x), "s"(step), "v"(v0), "v"(v1), "v"(v2), "v"(v3), "s"(ts), "i"(N)
               : "memory", "scc", "vcc");
}
template <int N> __device__ __forceinline__ void dma_w(v4u srd, u32 m0, u32 v0, u32 v1, u32 v2, u32 v3, u32 d0, u32 d1, u32 due, u32 ts) {
  asm volatile("s_mov_b32 m0, %8\nLU%=:\n\ts_memrealtime vcc\n\ts_waitcnt lgkmcnt(0)\n\ts_lshl_b32 vcc_lo, vcc_lo, 4\n\t"
               "s_sub_u32 vcc_lo, vcc_lo, %0\n\ts_cmp_lt_i32 vcc_lo, 0\n\ts_cbranch_scc1 LU%=\n\t"
               "buffer_load_dwordx4 %3, %7, %11 offen nt lds\n\tbuffer_load_dwordx4 %4, %7, %12 offen offset:1024 nt lds\n\t"
               "buffer_load_dwordx4 %5, %7, %13 offen offset:2048 nt lds\n\tbuffer_load_dwordx4 %6, %7, 0 offen offset:3072 nt lds\n\t"
               "s_add_u32 m0, m0, 0x1000\n\tv_xor_b32 %3, %1, %3\n\tv_xor_b32 %4, %1, %4\n\tv_xor_b32 %5, %1, %5\n\tv_xor_b32 %6, %1, %6\n\t"
               "s_mov_b32 vcc_lo, %1\n\ts_mov_b32 %1, %2\n\ts_mov_b32 %2, vcc_lo\n\ts_add_u32 %0, %0, %10\n\ts_cmp_lg_u32 m0, %9\n\ts_cbranch_scc1 LU%="
               : "+&s"(due), "+&s"(d0), "+&s"(d1), "+&v"(v0), "+&v"(v1), "+&v"(v2), "+&v"(v3)
               : "s"(srd), "s"(m0), "s"(m0 + N * 4096), "s"(ts), "s"(3072u), "s"(2048u), "s"(1024u) : "memory", "scc", "vcc");
}
__device__ __forceinline__ void dma_kv(v4u srd, u32 m0, u32 v0, u32 v1, u32 v2, u32 v3, u32 fprog, u32 fdt, u32 due, u32 tau, u64 dl) {
  u32 n = 0, d, prog = 0, dt;

  asm volatile("s_mov_b32 m0, %10\nLK%=:\n\ts_memrealtime vcc\n\tds_read_b32 %4, %12\n\t"
               "s_waitcnt lgkmcnt(0)\n\ts_lshl_b32 vcc_lo, vcc_lo, 4\n\t"
               "s_sub_u32 vcc_lo, vcc_lo, %2\n\ts_cmp_lt_i32 vcc_lo, 0\n\ts_cbranch_scc1 LK%=\n\t"
               "buffer_load_dwordx4 %5, %9, %15 offen nt lds\n\tbuffer_load_dwordx4 %6, %9, %16 offen offset:1024 nt lds\n\t"
               "buffer_load_dwordx4 %7, %9, %17 offen offset:2048 nt lds\n\tbuffer_load_dwordx4 %8, %9, 0 offen offset:3072 nt lds\n\t"
               "v_readfirstlane_b32 vcc_lo, %4\n\ts_add_u32 m0, m0, 0x1000\n\ts_add_u32 %1, %1, 1\n\ts_add_u32 %2, %2, vcc_lo\n\ts_ff1_i32_b32 %3, %1\n\ts_lshl_b32 %3, %3, 4\n\ts_lshr_b64 vcc, %18, %3\n\ts_and_b32 %3, vcc_lo, 0x7ff\n\ts_lshl_b32 %3, %3, 8\n\t"
               "v_xor_b32 %5, %3, %5\n\tv_xor_b32 %6, %3, %6\n\tv_xor_b32 %7, %3, %7\n\tv_xor_b32 %8, %3, %8\n\t"
               "s_add_u32 %2, %2, %14\n\tv_add_u32 %0, 1, %0\n\t"
               "s_cmp_lg_u32 %1, 16\n\ts_waitcnt vmcnt(%13)\n\t"
               "ds_write_b32 %11, %0\n\ts_cbranch_scc1 LK%=\n\ts_or_b32 %2, %2, 1\n\tv_mov_b32 %4, %2\n\tds_write_b32 %12, %4 offset:8"
               : "+&v"(prog), "+&s"(n), "+&s"(due), "=&s"(d), "=&v"(dt), "+&v"(v0), "+&v"(v1), "+&v"(v2), "+&v"(v3)
               : "s"(srd), "s"(m0), "v"(fprog), "v"(fdt), "i"(INFL), "s"(tau), "s"(3072u), "s"(2048u), "s"(1024u), "s"(dl)
               : "memory", "scc", "vcc");
}
template <int N0> __device__ __forceinline__ void dma_drain(u32 fprog, u32 p0, u32 p1, u32 p2) {
  asm volatile("s_waitcnt vmcnt(%4)\n\tds_write_b32 %0, %1\n\ts_waitcnt vmcnt(8)\n\tds_write_b32 %0, %2\n\ts_waitcnt vmcnt(0)\n\tds_write_b32 %0, %3"
               ::"v"(fprog), "v"(p0), "v"(p1), "v"(p2), "i"(N0) : "memory");
}
template <int X> __device__ __forceinline__ void vmwait() { asm volatile("s_waitcnt vmcnt(%0)" ::"i"(X) : "memory"); }
__device__ __forceinline__ void lds_put(u32 addr, u32 v) { asm volatile("ds_write_b32 %0, %1" ::"v"(addr), "v"(v) : "memory"); }
__device__ __forceinline__ u32 lds_get(u32 addr) {
  u32 v;
  asm volatile("ds_read_b32 %0, %1\n\ts_waitcnt lgkmcnt(0)" : "=v"(v) : "v"(addr) : "memory");
  return v;
}
template <int SLEEP> __device__ __forceinline__ void lds_wait(u32 addr, u32 need) {
  u32 v, cap = LSPIN_CAP;
  asm volatile("LW%=:\n\tds_read_b32 %0, %2\n\ts_waitcnt lgkmcnt(0)\n\tv_cmp_le_u32 vcc, %3, %0\n\ts_cmp_eq_u64 vcc, exec\n\ts_cbranch_scc1 LX%=\n\t"
               "s_sleep %4\n\ts_sub_u32 %1, %1, 1\n\ts_cbranch_scc0 LW%=\nLX%=:"
               : "=&v"(v), "+&s"(cap) : "v"(addr), "s"(need), "i"(SLEEP) : "memory", "vcc", "scc");
}

__global__ void __launch_bounds__(768, 3) __attribute__((aligned(4096)))
fused_decode(const char* __restrict__ wq, const char* __restrict__ x, const char* __restrict__ gw, u64* __restrict__ ws, const char* __restrict__ kc,
             const char* __restrict__ w1, const char* __restrict__ w3, const char* __restrict__ vc, const char* __restrict__ wo, const char* __restrict__ w2,
             u16* __restrict__ out) {
  __shared__ __attribute__((aligned(16))) char lds[L_END];
#define MIX(V, n, x) ({ u32 c_ = 0; _Pragma("unroll") for (int i_ = 0; i_ < n; ++i_) c_ ^= (0u - (((u32)(x) >> i_) & 1u)) & V(i_); c_; })
  const int l = threadIdx.x & 63, wv = __builtin_amdgcn_readfirstlane(threadIdx.x >> 6);
  const int G = blockIdx.x & 7, r = blockIdx.x >> 3, rh = r >> 3, rl = r & 7;
  const int g = l >> 4, c4 = (l & 15) >> 2, G2 = G ^ 4, h2 = 4 * G2 + rh, GQ = G2 ^ MIX(QO, 5, rh | rl << 2), rf = rl ^ 2;
  asm volatile("" ::"v"((LAS char*)lds));
  u32 codew = 0;
  if (wv == 2) {
    const u64 pc = __builtin_amdgcn_s_getpc() & ~127ull;
    asm volatile("global_load_dword %0, %1, %2" : "=v"(codew) : "v"(min(r + 32 * (l & 3), CODE_LINES - 1) * 128), "s"(pc) : "memory");
  }
  if (wv == 5) lds_put(L_FL + l * 4, 0u);
  const u32 wsw = ((l & 7) ^ ((g & 1) << 1)) << 4;
  u32 q0 = (l >> 3) * 8192 + wsw, q1 = (q0 ^ 64) + 64512, q2 = q0 + 2 * 64512, q3 = (q0 ^ 64) + 3 * 64512, l16 = l * 16;
  u32 qoff = 128 * rh * 8192 + (8 * GQ + rl) * 128;
  asm volatile("" : "+v"(q0), "+v"(q1), "+v"(q2), "+v"(q3), "+v"(l16));
  asm volatile("" : "+s"(qoff));
  SB();
  asm volatile("" ::"s"(x), "s"(kc), "s"(wq), "s"(w1), "s"(w3), "s"(gw), "s"(ws), "s"(vc), "s"(wo), "s"(w2), "s"(out));
  u64 t0q;
  asm volatile("s_memrealtime %0" : "=s"(t0q)::"memory");
  u32 epoch_v = 0;
  if (wv == 2) {
    dma4p(mksrd(x, 4096), L_X, l16);
    dma1p(mksrd(gw, 1024), L_X + 4096, l16);
    asm volatile("global_load_dword %0, %1, %2 sc1" : "=v"(epoch_v) : "v"(0u), "s"(ws) : "memory");
  }
  SB();
  if (wv == 0) {
    const v4u rs = mksrd(wq + qoff, 127u * 8192u + 128u);
    dma4(rs, L_WQ, 0, q0, q1, q2, q3);
    dma4(rs, L_WQ + 4096, 32 * 8192, q0, q1, q2, q3);
    dma4(rs, L_WQ + 8192, 64 * 8192, q0, q1, q2, q3);
    dma4(rs, L_WQ + 12288, 96 * 8192, q0, q1, q2, q3);
  }
  SB();
  asm volatile("s_waitcnt lgkmcnt(0)" : "+s"(t0q)::"memory");
  if (wv == 1 && r == 0) gst(ws + (WS_TS + (l & 7) * 16 + G), t0q);
  SB();
  asm volatile("s_barrier" ::: "memory");
  const u32 t0 = (u32)t0q * 16u;
  auto tend = [&]() -> u32 { const u32 te = __builtin_amdgcn_readfirstlane(lds_get(F_TEND)); return te ? te : t0 + T36; };
  const size_t kvoff = (size_t)((rh * 8 + G) * 8192 + 6144) * 256;
  const u32 kvc = MIX(VG, 2, g) ^ MIX(VR, 3, rl) ^ MIX(VP, 5, rh * 8 + G), kvs = ((l & 15) ^ (g << 2)) << 4;
  const u32 v0 = kvc * 256 + kvs, v1 = (kvc ^ VG(2)) * 256 + (kvs ^ 16), v2 = (kvc ^ VG(3)) * 256 + (kvs ^ 32), v3 = (kvc ^ VG(2) ^ VG(3)) * 256 + (kvs ^ 48);
  const u64 kvd = (u64)VN(0) | (u64)(VN(0) ^ VN(1)) << 16 | (u64)(VN(0) ^ VN(1) ^ VN(2)) << 32 | (u64)(VN(0) ^ VN(1) ^ VN(2) ^ VN(3)) << 48;
  const u32 woc = MIX(WO, 8, rl | h2 << 3), w2c = MIX(UO, 8, rl | h2 << 3);
  if (wv == 3) {
    __builtin_amdgcn_s_setprio(3);
    const v4u rs = mksrd(((rf >> 2) ? w3 : w1) + (size_t)(128 * rh * 3584 + (4 * (G < 7 ? G : 0) + (rf & 3)) * 128), G < 7 ? 127u * 3584u + 128u : 0u);
    const u32 b0 = (l >> 3) * 3584 + wsw, st8 = 8 * 3584 - 1024;
    dma_run<4>(rs, L_W13, __builtin_amdgcn_readfirstlane(MIX(XO, 8, rl | G << 3 | rh << 6)), 32 * 3584, b0, (b0 ^ 64) + st8, b0 + 2 * st8, (b0 ^ 64) + 3 * st8, t0 + T_K, TAU13);
  }
  SB();
  if (wv == 1) {
    __builtin_amdgcn_s_setprio(3);
    dma_kv(mksrd(kc + kvoff - 3072, 524288 + 3072), L_K, v0, v1, v2, v3, F_PK, F_DT, t0 + T4, TAU, kvd);
    dma_drain<16>(F_PK, LAG + 12, LAG + 14, LAG + 16);
  }
  SB();
  if (wv == 2) {
    asm volatile("s_waitcnt vmcnt(0)" : "+v"(epoch_v), "+v"(codew)::"memory");
    lds_put(F_TAG, epoch_v + 1u);
    lds_put(F_X, 1u);
  }
  SB();
  const u32 tr0 = (8 * g + c4) * 128 + (l & 1) * 8, trx = ((l >> 1) & 1) ^ ((c4 & 2) | ((g & 1) << 2));
  if (wv == 0) {
    lds_wait<1>(F_X, 1u);
    const u32 tag = __builtin_amdgcn_readfirstlane(lds_get(F_TAG));
    u32 wa = L_WQ + tr0 + (trx << 4);
    u64* pub = ws + (WS_Q + (((GQ * 4 + g) * 4 + rh) * 8 + rl) * 32 + (l & 15));
    v4u xf[4];
#pragma unroll
    for (int ks = 0; ks < 4; ++ks) xf[ks] = lds128(L_X + c4 * 1024 + rh * 256 + g * 16 + ks * 64);
#pragma unroll 1
    for (u32 pass = 1; pass < 3; ++pass) {
      u32 wb = wa ^ 32, wc = wa ^ 64, wd = wa ^ 96;
      PIN(wa); PIN(wb); PIN(wc); PIN(wd); PIN(pub);
      f32x4 d0 = {0.f, 0.f, 0.f, 0.f}, d1 = d0, d2 = d0, d3 = d0;
#pragma unroll
      for (int hf = 0; hf < 2; ++hf) {
        if (hf == 0) vmwait<8>(); else if (pass == 1) vmwait<0>();
        v4u f[8];
#pragma unroll
        for (int k = 0; k < 2; ++k) {
          const u32 o = (hf * 2 + k) * 4096;
          f[4 * k] = lds_tr(wa + o, wa + o + 512); f[4 * k + 1] = lds_tr(wb + o, wb + o + 512);
          f[4 * k + 2] = lds_tr(wc + o, wc + o + 512); f[4 * k + 3] = lds_tr(wd + o, wd + o + 512);
        }
        SB();
#pragma unroll
        for (int k = 0; k < 2; ++k) {
          d0 = mfma(xf[hf * 2 + k], f[4 * k], d0); d1 = mfma(xf[hf * 2 + k], f[4 * k + 1], d1);
          d2 = mfma(xf[hf * 2 + k], f[4 * k + 2], d2); d3 = mfma(xf[hf * 2 + k], f[4 * k + 3], d3);
        }
        SB();
      }
      lds_put(F_FREE, pass);
      lds_wait<1>(F_W13, 1u);
      gst(pub, gran(pk_bf16(d0[0], d1[0]), tag));
      gst(pub + 16, gran(pk_bf16(d2[0], d3[0]), tag));
      wa += L_W13 - L_WQ;
      pub = ws + (WS_H + ((((G * 4 + (rf & 3)) * 4 + g) * 4 + rh) * 2 + (rf >> 2)) * 32 + (l & 15));
    }
    if (blockIdx.x == 3) {
      u64 ge[4];
      lds_wait<4>(F_PV, 16u);
      sweep<4>(ge, ws + (WS_Q + (l >> 5) * 4096 + (l & 31) * 32), 8192, tag);
      __hip_atomic_store((GAS u32*)ws, tag, RLX_AGENT);
    }
    EXIT();
  }
  if (wv == 1) {
    const v4u ro = mksrd(wo + (size_t)h2 * 131072 + rl * 128 - 3072, 131072 - rl * 128 + 3072);
    const v4u r2 = mksrd(w2 + (size_t)(G2 < 7 ? h2 : 0) * 65536 + rl * 128 - 3072, G2 < 7 ? 65536 - rl * 128 + 3072 : 0);
    const u32 a0 = (l >> 3) * 1024 + wsw, a1 = a0 ^ 64;
    const u32 tw = tend() + WGAP;
    lds_wait<1>(F_FREE, 2u);
    lds_wait<1>(F_G, 1u);
    lds_wait<1>(F_PV, 16u);
    dma_w<2>(r2, L_W + 16384, a0 + w2c * 8192, a1 + (w2c ^ UG(0)) * 8192, a0 + (w2c ^ UG(1)) * 8192, a1 + (w2c ^ UG(0) ^ UG(1)) * 8192, UG(2) * 8192, 0, tw, TAUW);
    dma_w<4>(ro, L_W, a0 + woc * 8192, a1 + (woc ^ WG(0)) * 8192, a0 + (woc ^ WG(1)) * 8192, a1 + (woc ^ WG(0) ^ WG(1)) * 8192, WG(2) * 8192, (WG(2) ^ WG(3)) * 8192,
             tw + 2 * TAUW, TAUW);
    dma_drain<16>(F_W, 1u, 2u, 3u);
    EXIT();
  }
  if (wv == 3) {
    vmwait<0>();
    lds_put(F_W13, 1u);
    lds_wait<4>(F_PK, 14u);
    lds_wait<1>(F_FREE, 1u);
    const u32 te = tend();
    lds_wait<0>(F_DUE, 1u);
    const u32 due = __builtin_amdgcn_readfirstlane(lds_get(F_DUE));
    dma_kv(mksrd(vc + kvoff - 3072, 524288 + 3072), L_V, v0, v1, v2, v3, F_PV, F_ZERO, due, max((int)(te - due) >> 4, TAU - DTMAX), kvd);
    dma_drain<24>(F_PV, LAG + 10, LAG + 14, LAG + 16);
    EXIT();
  }
  if (wv == 2) {
    const u32 tag = __builtin_amdgcn_readfirstlane(epoch_v) + 1u;
    float gate;
    {
      float s = 0.f;
#pragma unroll 1
      for (int k = 0; k < 4; ++k) {
        const v4u a = lds128(L_X + g * 1024 + k * 256 + (l & 15) * 16), b = lds128(L_X + 4096 + k * 256 + (l & 15) * 16);
#pragma unroll
        for (int d = 0; d < 4; ++d) {
          const u32 pr = pk_bf16(asf(a[d] << 16) * asf(b[d] << 16), asf(a[d] & 0xffff0000u) * asf(b[d] & 0xffff0000u));
          s += asf(pr << 16) + asf(pr & 0xffff0000u);
        }
      }
      s += dpp<0xB1>(s); s += dpp<0x4E>(s); s += dpp<0x141>(s); s += dpp<0x128>(s);
      gate = rbf(__builtin_amdgcn_rcpf(1.f + __expf(-rbf(s))));
      lds_put(L_GT + g * 4, asu(gate));
      lds_put(F_G, 1u);
    }
    u64* mpub = ws + (WS_ML + ((G * 4 + rh) * 8 + rl) * 16 + g * 2 + (l & 1));
    const u64* mp = ws + (WS_ML + ((G2 * 4 + g) * 8 + ((l & 15) >> 1)) * 16 + rh * 2 + (l & 1));
    PIN(mpub); PIN(mp);
    lds_wait<1>(F_P + (l & 7) * 4, 1u);
    {
      const v4u ma = lds128(L_MX + g * 32), mb = lds128(L_MX + g * 32 + 16), la = lds128(L_LS + g * 32), lb = lds128(L_LS + g * 32 + 16);
      const float M = max8(ma, mb);
      float lsum = 0.f;
#pragma unroll
      for (int k = 0; k < 4; ++k) lsum = fmaf(expd(asf(ma[k]), M), asf(la[k]), fmaf(expd(asf(mb[k]), M), asf(lb[k]), lsum));
      gst(mpub, gran(asu((l & 1) ? lsum : M), tag));
    }
    const u32 tm = tend() + ODELAY - MLEAD;
    lds_wait<1>(F_PV, 16u);
    u64 gm;
#pragma unroll 1
    for (u32 spins = 0; spins < SPIN_CAP; ++spins) {
      gm = gld(mp);
      if (tags_ok<1>(&gm, tag)) break;
      until<1>(tm);
    }
    const float v = asf((u32)gm);
    const float ms = dpp<0xA0>(v), lsv = dpp<0xF5>(v);
    float M = fmaxf(ms, dpp<0x4E>(ms)); M = fmaxf(M, dpp<0x124>(M)); M = fmaxf(M, dpp<0x128>(M));
    const float e = expd(ms, M);
    float L = e * lsv; L += dpp<0x4E>(L); L += dpp<0x124>(L); L += dpp<0x128>(L);
    lds_put(L_WT + g * 32 + (l & 2) * 8 + (l & 12), asu(e * gate * __builtin_amdgcn_rcpf(L)));
    lds_put(F_WT, 1u);
    EXIT();
  }
  const int w = wv - 4, ct = w & 3;
  if (w == 7) {
    float slot = 4.f, tau = TAU, tprev = T4;
#pragma unroll 1
    for (int pass = 0; pass < 2; ++pass) {
      until<1>(t0 + (pass ? T_S1 : T_S0));
      const u64 now = __builtin_amdgcn_s_memrealtime();
      const u64 tj = gld(ws + (WS_TS + G * 16 + (l & 7)));
      const bool ok = now - tj < 400ull;
      float d = 16.f * (float)(int)(u32)((ok ? tj : pass ? now - 40ull : t0q) - t0q), dm = d;
      d += dpp<0xB1>(d); d += dpp<0x4E>(d); d += dpp<0x141>(d);
      dm = fmaxf(dm, dpp<0xB1>(dm)); dm = fmaxf(dm, dpp<0x4E>(dm)); dm = fmaxf(dm, dpp<0x141>(dm));
      const float te = fminf(fmaxf(0.125f * d, dm - fminf(CMAX + (CSL * 0.01f) * (dm - 0.125f * d), DTMAX * 28)), 4000.f) + T36;
      const float tn = fmaxf((float)(int)(clk16() - t0), T4);
      slot += (tn - tprev) * __builtin_amdgcn_rcpf(tau);
      tprev = tn;
      tau = fminf(fmaxf((te - tn) * __builtin_amdgcn_rcpf(36.f - slot), TAU - DTMAX), 2 * TAU);
      lds_put(F_DT, (u32)((int)tau - TAU));
      lds_put(F_TEND, (t0 + (u32)(int)te) | 1u);
      if (__all(ok)) break;
    }
  }
  lds_wait<1>(F_X, 1u);
  const u32 tag = __builtin_amdgcn_readfirstlane(lds_get(F_TAG));
  u64* pub = ws + ((w < 4 ? WS_O + (((G * 4 + g) * 4 + rh) * 8 + rl) * 64 + ct * 16 : WS_R + (((rl * 4 + g) * 4 + ct) * 32 + h2) * 16) + (l & 15));
  const u32 p4 = ((l >> 3) & 3) * 64 + (l & 7) * 4 + (l >> 5) * 32;
  v4u g2[2];
  {
    const __amdgpu_buffer_rsrc_t qrs = __builtin_amdgcn_make_buffer_rsrc(ws + (WS_Q + (G * 4 + rh) * 1024 + ct * 64), 0, 8192, 0x00020000);
    const u32 qo = (l >> 5) * 2048 + (l & 31) * 16;
    if (w < 4) {
      lds_wait<1>(F_PK, QPROG);
      fetch2<2>(g2, qrs, qo, 4096);
    }
    SB();
    u32 qa = L_QS + c4 * QSTR + g * 16;
    const u32 ka = L_K + w * 8192 + (l & 15) * 256 + ((g ^ c4 ^ ((l & 3) << 2)) << 4);
    v4u kf[8];
    lds_wait<1>(F_PK, LAG + 2 * w + 2);
#pragma unroll
    for (int ks = 0; ks < 4; ++ks) { kf[2 * ks] = lds128(ka ^ (ks << 6)); kf[2 * ks + 1] = lds128((ka ^ (ks << 6)) + 4096); }
#pragma unroll
    for (int k = 0; k < 8; ++k) PIN(kf[k]);
    u32 amw = L_MX + c4 * 32 + w * 4, afw = F_P + w * 4, apw = L_P + c4 * PSTR + w * 64 + (l & 1) * 32 + g * 8;
    PIN(qa); PIN(amw); PIN(afw); PIN(apw);
    if (w < 4) {
      if (!fits2<2>(g2, tag)) sweep2<2>(g2, qrs, qo, 4096, tag);
      lds_put(L_QS + w * QSTR + p4, pk_bf16(halves(asf(g2[0][0] << 16) + asf(g2[1][0] << 16), asf(g2[0][0] & 0xffff0000u) + asf(g2[1][0] & 0xffff0000u)),
                                            halves(asf(g2[0][2] << 16) + asf(g2[1][2] << 16), asf(g2[0][2] & 0xffff0000u) + asf(g2[1][2] & 0xffff0000u))));
      lds_put(F_Q + w * 4, 1u);
    }
    lds_wait<1>(F_Q + (l & 3) * 4, 1u);
    v4u qf[4];
#pragma unroll
    for (int ks = 0; ks < 4; ++ks) qf[ks] = lds128(qa + ks * 64);
    SB();
    f32x4 s0 = {0.f, 0.f, 0.f, 0.f}, s1 = {0.f, 0.f, 0.f, 0.f};
#pragma unroll
    for (int ks = 0; ks < 4; ++ks) { s0 = mfma(kf[2 * ks], qf[ks], s0); s1 = mfma(kf[2 * ks + 1], qf[ks], s1); }
    float m = fmaxf(fmaxf(fmaxf(s0[0], s0[1]), fmaxf(s0[2], s0[3])), fmaxf(fmaxf(s1[0], s1[1]), fmaxf(s1[2], s1[3])));
    m = x16_32_max(m);
    const bool t1 = l & 1;
    float p[4], ls = 0.f;
#pragma unroll
    for (int i = 0; i < 4; ++i) { p[i] = expd(t1 ? s1[i] : s0[i], m); ls += p[i]; }
    *(LAS v2u*)(size_t)apw = v2u{pk_bf16(p[0], p[1]), pk_bf16(p[2], p[3])};
    lds_put(amw, asu(m));
    ls += dpp<0xB1>(ls);
    ls = x16_32_sum(ls);
    lds_put(amw + 128, asu(ls));
    lds_put(afw, 1u);
  }
  const u32 tsweep = tend() + ODELAY;
  if (w < 4) {
    const __amdgpu_buffer_rsrc_t ors = __builtin_amdgcn_make_buffer_rsrc(ws + (WS_O + (h2 * 4 + w) * 512), 0, 4096, 0x00020000);
    u32 aow = L_OA + w * OSTR + p4;
    const u64* rp = ws + (WS_R + ((rl * 16 + (h2 >> 1)) * 32 + (l >> 3)) * 16 + (h2 & 1) * 8 + (l & 7));
    u16* outp = out + ((h2 >> 3) * 512 + rl * 64 + (h2 & 7) * 8 + (l & 7));
    PIN(aow); PIN(rp); PIN(outp);
    lds_wait<0>(F_P + (l & 7) * 4, 1u);
    {
      const v4u ma = lds128(L_MX + g * 32), mb = lds128(L_MX + g * 32 + 16);
      const float M = max8(ma, mb);
      float cf[8];
#pragma unroll
      for (int k = 0; k < 4; ++k) { cf[k] = expd(asf(ma[k]), M); cf[4 + k] = expd(asf(mb[k]), M); }
      v4u pf[8];
#pragma unroll
      for (int ks = 0; ks < 8; ++ks) pf[ks] = lds128(L_P + c4 * PSTR + ks * 64 + g * 16);
      const u32 ch = 4 * w + ((l >> 1) & 1);
      u32 va0 = L_V + (8 * g + c4) * 256 + ((ch ^ ((c4 << 2) | ((2 * g) & 3))) << 4) + (l & 1) * 8;
      u32 va1 = (va0 ^ 16) + 1024, vb0 = va0 ^ 32, vb1 = va1 ^ 32;
      PIN(va0); PIN(va1); PIN(vb0); PIN(vb1);
#pragma unroll
      for (int k = 0; k < 8; ++k) PIN(cf[k]);
      const f32x4 z = {0.f, 0.f, 0.f, 0.f};
      float o0 = 0.f, o1 = 0.f;
      auto steps = [&](auto K0, auto K1) {
        constexpr int n = K1.value - K0.value;
        v4u f[2 * n];
#pragma unroll
        for (int i = 0; i < n; ++i) {
          const u32 o = (K0.value + i) * 8192;
          f[2 * i] = lds_tr(va0 + o, va1 + o); f[2 * i + 1] = lds_tr(vb0 + o, vb1 + o);
        }
        SB();
        f32x4 d[2 * n];
#pragma unroll
        for (int i = 0; i < n; ++i) { d[2 * i] = mfma(pf[K0.value + i], f[2 * i], z); d[2 * i + 1] = mfma(pf[K0.value + i], f[2 * i + 1], z); }
        SB();
#pragma unroll
        for (int i = 0; i < n; ++i) { o0 = fmaf(cf[K0.value + i], d[2 * i][0], o0); o1 = fmaf(cf[K0.value + i], d[2 * i + 1][0], o1); }
        PIN(o0); PIN(o1);
      };
      lds_wait<1>(F_PV, LAG + 6);
      steps(IC<0>{}, IC<3>{});
      lds_wait<0>(F_PV, LAG + 10);
      steps(IC<3>{}, IC<5>{});
      lds_wait<0>(F_PV, LAG + 14);
      steps(IC<5>{}, IC<7>{});
      lds_wait<0>(F_PV, LAG + 16);
      steps(IC<7>{}, IC<8>{});
      gst(pub, gran(pk_bf16(o0, o1), tag));
      SB();
    }
    {
      v4u go[4];
      until<1>(tsweep);
      fetch2<4>(go, ors, l16, 1024);
      SB();
      lds_wait<0>(F_WT, 1u);
      v4u wk = lds128(L_WT + w * 32 + (l >> 5) * 16);
      PIN(wk);
      SB();
      auto combine = [&]() -> u32 {
        float a0 = 0.f, a1 = 0.f, b0 = 0.f, b1 = 0.f;
#pragma unroll
        for (int k = 0; k < 4; ++k) {
          a0 = fmaf(asf(wk[k]), asf(go[k][0] << 16), a0); b0 = fmaf(asf(wk[k]), asf(go[k][0] & 0xffff0000u), b0);
          a1 = fmaf(asf(wk[k]), asf(go[k][2] << 16), a1); b1 = fmaf(asf(wk[k]), asf(go[k][2] & 0xffff0000u), b1);
        }
        return pk_bf16(halves(a0, b0), halves(a1, b1));
      };
      lds_put(aow, combine());
      if (!fits2<4>(go, tag)) {
        sweep2<4>(go, ors, l16, 1024, tag);
        lds_put(aow, combine());
      }
      BAR();
    }
    if (w == 0) {
      u64 gr[4];
      const u32 tb = clk16() + RDELAY, tr = tsweep + RABS;
      until<0>((int)(tb - tr) > 0 ? tb : tr);
      sweep<4>(gr, rp, 128, tag);
      float s = (asf((u32)gr[0]) + asf((u32)gr[1])) + (asf((u32)gr[2]) + asf((u32)gr[3]));
      s += dpp<0x128>(s);
      s = x16_32_sum(s);
      if (l < 8) *(GAS u16*)outp = (u16)pk_bf16(s, s);
    }
  } else {
    lds_wait<1>(F_PV, HPROG);
    sweep2<2>(g2, __builtin_amdgcn_make_buffer_rsrc(ws + (WS_H + (h2 * 4 + ct) * 256), 0, 2048, 0x00020000), l16, 1024, tag);
    const float a = rbf(halves(asf(g2[0][0] << 16) + asf(g2[1][0] << 16), asf(g2[0][0] & 0xffff0000u) + asf(g2[1][0] & 0xffff0000u)));
    const float b2 = rbf(halves(asf(g2[0][2] << 16) + asf(g2[1][2] << 16), asf(g2[0][2] & 0xffff0000u) + asf(g2[1][2] & 0xffff0000u)));
    auto sa = __builtin_amdgcn_permlane16_swap(asu(a), asu(a), false, false);
    auto sb = __builtin_amdgcn_permlane16_swap(asu(b2), asu(b2), false, false);
    const float ga = asf(sa[0]), ua = asf(sa[1]), gb = asf(sb[0]), ub = asf(sb[1]);
    lds_wait<0>(F_G, 1u);
    const float og = 1.f - asf(lds_get(L_GT + ct * 4));
    const float ha = rbf(rbf(ga * __builtin_amdgcn_rcpf(1.f + __expf(-ga))) * ua) * og, hb = rbf(rbf(gb * __builtin_amdgcn_rcpf(1.f + __expf(-gb))) * ub) * og;
    lds_put(L_HA + ct * HSTR + (p4 & 127), pk_bf16(ha, hb));
    lds_put(F_H + ct * 4, 1u);
    lds_wait<0>(F_H + (l & 3) * 4, 1u);
    const u32 hb8 = w2c ^ MIX(UG, 2, g);
    const v4u h0 = lds128(L_HA + c4 * HSTR + hb8 * 16), h1 = lds128(L_HA + c4 * HSTR + (hb8 ^ UG(2)) * 16);
    u32 wa = L_W + tr0 + ((trx ^ (2 * ct)) << 4), ok[4];
#pragma unroll
    for (int j = 0; j < 4; ++j) { ok[j] = L_OA + c4 * OSTR + (woc ^ MIX(WG, 4, g | j << 2)) * 16; PIN(ok[j]); }
    PIN(wa);
    f32x4 acc = {0.f, 0.f, 0.f, 0.f};
    v4u bw[4];
    lds_wait<1>(F_W, 1u);
    if (G2 < 7) {
      acc = mfma(h0, lds_tr(wa + 16384, wa + 16384 + 512), acc);
      acc = mfma(h1, lds_tr(wa + 20480, wa + 20480 + 512), acc);
    }
    lds_wait<0>(F_W, 2u);
    bw[0] = lds_tr(wa, wa + 512); bw[1] = lds_tr(wa + 4096, wa + 4096 + 512);
    lds_wait<0>(F_W, 3u);
    bw[2] = lds_tr(wa + 8192, wa + 8192 + 512); bw[3] = lds_tr(wa + 12288, wa + 12288 + 512);
    BAR();
    v4u of[4];
#pragma unroll
    for (int ks = 0; ks < 4; ++ks) of[ks] = lds128(ok[ks]);
    SB();
    f32x4 ac2 = mfma(of[1], bw[1], f32x4{0.f, 0.f, 0.f, 0.f});
    acc = mfma(of[0], bw[0], acc);
    ac2 = mfma(of[3], bw[3], ac2);
    acc = mfma(of[2], bw[2], acc);
    gst(pub, gran(asu(acc[0] + ac2[0]), tag));
  }
}

extern "C" void kernel_launch(void* const* d_in, const int* in_sizes, int n_in, void* d_out, int out_size, void* d_ws, size_t ws_size,
                              hipStream_t stream) {
  fused_decode<<<256, 768, 0, stream>>>((const char*)d_in[3], (const char*)d_in[0], (const char*)d_in[9], (u64*)d_ws, (const char*)d_in[1], (const char*)d_in[6],
                                        (const char*)d_in[7], (const char*)d_in[2], (const char*)d_in[4], (const char*)d_in[8], (u16*)d_out);
}
```
